```python
import torch

def _softmax(x, axis=-1):
    return torch.softmax(x, dim=axis)
def _gelu(x):
    return torch.nn.functional.gelu(x, approximate="tanh")
def _silu(x):
    return torch.nn.functional.silu(x)
def _sigmoid(x):
    return torch.sigmoid(x)
def _layernorm(x, axis=-1, eps=1e-5):
    m = x.mean(dim=axis, keepdim=True)
    v = x.var(dim=axis, keepdim=True, unbiased=False)
    return (x - m) / torch.sqrt(v + eps)
def _scatter_add(tgt, idx, src, axis=0):
    return tgt.clone().index_add_(axis, idx, src)
def _segment_sum(src, seg, n):
    out = torch.zeros((n,) + src.shape[1:], dtype=src.dtype)
    return out.index_add_(0, seg, src)
def _pad(x, pw):
    flat = tuple(v for pair in reversed(pw) for v in pair)
    return torch.nn.functional.pad(x, flat)

kernel_name = 'standalone__linear_kernel_rfa__decode'

def setup_inputs():

    _w = (lambda a: (lambda t: t.to(torch.bfloat16) if t.is_floating_point() else t)(torch.as_tensor(a)))
    B, H, Sq, Sk, D, M = 2, 32, 1, 4096, 128, 8
    return {
        "q": _w((lambda *s: torch.randn(*s, dtype=torch.bfloat16) * 0.4)(B, H, Sq, D)),
        "k": _w((lambda *s: torch.randn(*s, dtype=torch.bfloat16) * 1.6)(B, H, Sk, D)),
        "v": _w((lambda *s: torch.randn(*s, dtype=torch.bfloat16) * 1.6)(B, H, Sk, D)),
        "omega": _w((lambda *s: torch.randn(*s, dtype=torch.bfloat16) * 1.6)(M, D)),
    }

def reference(q, k, v, omega):
    d = q.shape[-1]
    m = omega.shape[0]
    scale = float(d) ** -0.25
    qs = q * scale
    ks = k * scale
    q_proj = torch.einsum("bhsd,md->bhsm", qs, omega)
    k_proj = torch.einsum("bhsd,md->bhsm", ks, omega)
    q_sq = 0.5 * torch.einsum("bhsd,bhsd->bhs", qs, qs)[..., None]
    k_sq = 0.5 * torch.einsum("bhsd,bhsd->bhs", ks, ks)[..., None]
    inv_sqrt_m = float(m) ** -0.5
    phi_q = torch.exp(q_proj - q_sq) * inv_sqrt_m + 1e-6
    phi_k = torch.exp(k_proj - k_sq) * inv_sqrt_m + 1e-6
    kv = torch.einsum("bhsm,bhsd->bhmd", phi_k, v)
    z  = torch.einsum("bhsm->bhm", phi_k)
    num = torch.einsum("bhqm,bhmd->bhqd", phi_q, kv)
    den = torch.einsum("bhqm,bhm->bhq", phi_q, z)[..., None] + 1e-6
    return num / den
```

```cpp
#include <hip/hip_runtime.h>
#include <hip/hip_bf16.h>
#include <stdint.h>

#define MODE 0

typedef unsigned int u32;
using u32x4 = __attribute__((ext_vector_type(4))) unsigned;

constexpr int NB = 2, NH = 32, SK = 4096, D = 128, M = 8;
constexpr int NSPLIT = 8;
constexpr int KEYS_PER_WG = SK / NSPLIT;

__device__ __forceinline__ float bf_lo(u32 x) { return __uint_as_float(x << 16); }
__device__ __forceinline__ float bf_hi(u32 x) { return __uint_as_float(x & 0xffff0000u); }
__device__ __forceinline__ float rbf(float x) {
  return x;
}

__global__ void __launch_bounds__(256) rfa_split(const __hip_bfloat16* __restrict__ k, const __hip_bfloat16* __restrict__ v,
                                                 const __hip_bfloat16* __restrict__ omega, float* __restrict__ ws) {
  __shared__ float red[4][M * D + M];
  const int split = blockIdx.x, bh = blockIdx.y;
  const int tid = threadIdx.x, lane = tid & 63, wave = tid >> 6, r = lane >> 4, c = lane & 15;
  const float scale = 0.29730177875068026f;
  float om[M][8];
#pragma unroll
  for (int m = 0; m < M; ++m) {
    u32x4 o = *reinterpret_cast<const u32x4*>(omega + m * D + 8 * c);
#pragma unroll
    for (int j = 0; j < 4; ++j) { om[m][2 * j] = bf_lo(o[j]); om[m][2 * j + 1] = bf_hi(o[j]); }
  }
  float acc[M][8];
  float z[M];
#pragma unroll
  for (int m = 0; m < M; ++m) { z[m] = 0.f;
#pragma unroll
    for (int j = 0; j < 8; ++j) acc[m][j] = 0.f; }
  const size_t base = ((size_t)bh * SK + (size_t)split * KEYS_PER_WG) * D;
  for (int it = 0; it < KEYS_PER_WG / 16; ++it) {
    const int key = it * 16 + wave * 4 + r;
    u32x4 kk = *reinterpret_cast<const u32x4*>(k + base + (size_t)key * D + 8 * c);
    u32x4 vv = *reinterpret_cast<const u32x4*>(v + base + (size_t)key * D + 8 * c);
    float kf[8], vf[8];
#pragma unroll
    for (int j = 0; j < 4; ++j) {
      kf[2 * j] = rbf(bf_lo(kk[j]) * scale); kf[2 * j + 1] = rbf(bf_hi(kk[j]) * scale);
      vf[2 * j] = bf_lo(vv[j]); vf[2 * j + 1] = bf_hi(vv[j]);
    }
    float p[M + 1];
#pragma unroll
    for (int m = 0; m < M; ++m) { float s = 0.f;
#pragma unroll
      for (int j = 0; j < 8; ++j) s = fmaf(kf[j], om[m][j], s);
      p[m] = s; }
    { float s = 0.f;
#pragma unroll
      for (int j = 0; j < 8; ++j) s = fmaf(kf[j], kf[j], s);
      p[M] = s; }
#pragma unroll
    for (int m = 0; m <= M; ++m) {
#pragma unroll
      for (int o = 1; o < 16; o <<= 1) p[m] += __shfl_xor(p[m], o);
    }
    const float ksq = 0.5f * rbf(p[M]);
#pragma unroll
    for (int m = 0; m < M; ++m) {
      float x = rbf(rbf(p[m]) - ksq);
      float e = rbf(expf(x));
      float ph = rbf(rbf(e * 0.35355339059327373f) + 1e-6f);
      z[m] += ph;
#pragma unroll
      for (int j = 0; j < 8; ++j) acc[m][j] = fmaf(ph, vf[j], acc[m][j]);
    }
  }
#pragma unroll
  for (int m = 0; m < M; ++m) {
    z[m] += __shfl_xor(z[m], 16); z[m] += __shfl_xor(z[m], 32);
#pragma unroll
    for (int j = 0; j < 8; ++j) { acc[m][j] += __shfl_xor(acc[m][j], 16); acc[m][j] += __shfl_xor(acc[m][j], 32); }
  }
  if (r == 0) {
#pragma unroll
    for (int m = 0; m < M; ++m) {
#pragma unroll
      for (int j = 0; j < 8; ++j) red[wave][m * D + 8 * c + j] = acc[m][j];
      if (c == 0) red[wave][M * D + m] = z[m];
    }
  }
  __syncthreads();
  float* out = ws + ((size_t)bh * NSPLIT + split) * (M * D + M);
  for (int i = tid; i < M * D + M; i += 256) out[i] = red[0][i] + red[1][i] + red[2][i] + red[3][i];
}

__global__ void __launch_bounds__(128) rfa_combine(const __hip_bfloat16* __restrict__ q, const __hip_bfloat16* __restrict__ omega,
                                                   const float* __restrict__ ws, __hip_bfloat16* __restrict__ out) {
  const int bh = blockIdx.x, d = threadIdx.x;
  const float scale = 0.29730177875068026f;
  float phq[M];
  {
    float qsq = 0.f; float pr[M];
    for (int m = 0; m < M; ++m) pr[m] = 0.f;
    for (int j = 0; j < D; ++j) {
      float qs = rbf(__bfloat162float(q[bh * D + j]) * scale);
      qsq = fmaf(qs, qs, qsq);
      for (int m = 0; m < M; ++m) pr[m] = fmaf(qs, __bfloat162float(omega[m * D + j]), pr[m]);
    }
    qsq = 0.5f * rbf(qsq);
    for (int m = 0; m < M; ++m) {
      float x = rbf(rbf(pr[m]) - qsq);
      float e = rbf(expf(x));
      phq[m] = rbf(rbf(e * 0.35355339059327373f) + 1e-6f);
    }
  }
  float num = 0.f, den = 0.f;
  for (int m = 0; m < M; ++m) {
    float kv = 0.f, z = 0.f;
    for (int s = 0; s < NSPLIT; ++s) {
      const float* p = ws + ((size_t)bh * NSPLIT + s) * (M * D + M);
      kv += p[m * D + d]; z += p[M * D + m];
    }
    kv = rbf(kv); z = rbf(z);
    num = fmaf(phq[m], kv, num);
    den = fmaf(phq[m], z, den);
  }
  num = rbf(num);
  den = rbf(rbf(den) + 1e-6f);
  out[bh * D + d] = __float2bfloat16(num / den);
}

extern "C" void kernel_launch(void* const* d_in, const int* in_sizes, int n_in,
                              void* d_out, int out_size, void* d_ws, size_t ws_size,
                              hipStream_t stream) {
  const __hip_bfloat16* q = (const __hip_bfloat16*)d_in[0];
  const __hip_bfloat16* k = (const __hip_bfloat16*)d_in[1];
  const __hip_bfloat16* v = (const __hip_bfloat16*)d_in[2];
  const __hip_bfloat16* om = (const __hip_bfloat16*)d_in[3];
  rfa_split<<<dim3(NSPLIT, NB * NH), 256, 0, stream>>>(k, v, om, (float*)d_ws);
  rfa_combine<<<NB * NH, 128, 0, stream>>>(q, om, (const float*)d_ws, (__hip_bfloat16*)d_out);
}
```

```cpp
#include <hip/hip_runtime.h>
#include <hip/hip_bf16.h>
#include <stdint.h>

typedef unsigned int u32;
typedef unsigned long long u64;
using u32x2 = __attribute__((ext_vector_type(2))) unsigned;
using u32x4 = __attribute__((ext_vector_type(4))) unsigned;
using f32x2 = __attribute__((ext_vector_type(2))) float;
using f32x4 = __attribute__((ext_vector_type(4))) float;
using bf16x2 = __attribute__((ext_vector_type(2))) __bf16;
using bf16x8 = __attribute__((ext_vector_type(8))) __bf16;
using s16x4 = __attribute__((ext_vector_type(4))) short;
#define LAS __attribute__((address_space(3)))
#define GAS __attribute__((address_space(1)))

constexpr int NBH = 64, SK = 4096, D = 128, M = 8;
constexpr int NSPLIT = 4, NWAVES = 4;
constexpr int TILE_B = 4096;
constexpr int NSLOT = 8;
constexpr int TPW = SK / NSPLIT / 16 / NWAVES;
constexpr int WAVE_LDS = NSLOT * TILE_B + 4096;
constexpr int LDS_BYTES = NWAVES * WAVE_LDS;
constexpr int PART_F = 132;

__device__ __forceinline__ float bf_lo(u32 x) { return __uint_as_float(x << 16); }
__device__ __forceinline__ float bf_hi(u32 x) { return __uint_as_float(x & 0xffff0000u); }
__device__ __forceinline__ u32 pk_bf16(float a, float b) {
  f32x2 f = {a, b};
  return __builtin_bit_cast(u32, __builtin_convertvector(f, bf16x2));
}
__device__ __forceinline__ u32 scale_pk(u32 x, float s) {
  f32x2 f = {bf_lo(x), bf_hi(x)};
  f = f * s;
  return __builtin_bit_cast(u32, __builtin_convertvector(f, bf16x2));
}
__device__ __forceinline__ float dot2(u32 a, u32 b, float c) {
  return __builtin_amdgcn_fdot2_f32_bf16(__builtin_bit_cast(bf16x2, a), __builtin_bit_cast(bf16x2, b), c, false);
}
__device__ __forceinline__ f32x4 mfma32(u32x4 a, u32x4 b, f32x4 c) {
  return __builtin_amdgcn_mfma_f32_16x16x32_bf16(__builtin_bit_cast(bf16x8, a), __builtin_bit_cast(bf16x8, b), c, 0, 0, 0);
}
__device__ __forceinline__ f32x4 mfma4f(float a, float b, f32x4 c) { return __builtin_amdgcn_mfma_f32_16x16x4f32(a, b, c, 0, 0, 0); }

__device__ __forceinline__ void fill4k(const char* sbase, const u32 (&voff)[4], u32 lds) {
  u32 keep;
  asm volatile("s_mov_b32 %0, m0\n\ts_mov_b32 m0, %6\n\ts_nop 0\n\t"
               "global_load_lds_dwordx4 %1, %5 offset:0 nt\n\t"
               "global_load_lds_dwordx4 %2, %5 offset:1024 nt\n\t"
               "global_load_lds_dwordx4 %3, %5 offset:2048 nt\n\t"
               "global_load_lds_dwordx4 %4, %5 offset:3072 nt\n\t"
               "s_mov_b32 m0, %0"
               : "=&s"(keep) : "v"(voff[0]), "v"(voff[1]), "v"(voff[2]), "v"(voff[3]), "s"(sbase), "s"(lds) : "memory");
}
__device__ __forceinline__ void dma1k(const char* sbase, u32 voff, u32 lds) {
  u32 keep;
  asm volatile("s_mov_b32 %0, m0\n\ts_mov_b32 m0, %3\n\ts_nop 0\n\t"
               "global_load_lds_dwordx4 %1, %2\n\t"
               "s_mov_b32 m0, %0"
               : "=&s"(keep) : "v"(voff), "s"(sbase), "s"(lds) : "memory");
}
template <int N> __device__ __forceinline__ void wait_vm() { asm volatile("s_waitcnt vmcnt(%0)" :: "i"(N) : "memory"); }
__device__ __forceinline__ void wait_lgkm0() { asm volatile("s_waitcnt lgkmcnt(0)" ::: "memory"); }
#define PIN4(x) asm volatile("" : "+v"(x[0]), "+v"(x[1]), "+v"(x[2]), "+v"(x[3]) :: "memory")
#define PIN8(x) asm volatile("" : "+v"(x[0]), "+v"(x[1]), "+v"(x[2]), "+v"(x[3]), "+v"(x[4]), "+v"(x[5]), "+v"(x[6]), "+v"(x[7]) :: "memory")
__device__ __forceinline__ u32x4 lds_rd128(u32 addr) { return *(const LAS u32x4*)(size_t)addr; }
__device__ __forceinline__ u32x2 lds_tr(u32 addr) {
  return __builtin_bit_cast(u32x2, __builtin_amdgcn_ds_read_tr16_b64_v4i16((LAS s16x4*)(size_t)addr));
}

__device__ __forceinline__ u32x2 phi4(f32x4 pr, f32x4 sq) {
  const u32 p01 = pk_bf16(pr[0], pr[1]), p23 = pk_bf16(pr[2], pr[3]);
  const u32 s01 = pk_bf16(sq[0], sq[1]), s23 = pk_bf16(sq[2], sq[3]);
  const u32 x01 = pk_bf16(fmaf(-0.5f, bf_lo(s01), bf_lo(p01)), fmaf(-0.5f, bf_hi(s01), bf_hi(p01)));
  const u32 x23 = pk_bf16(fmaf(-0.5f, bf_lo(s23), bf_lo(p23)), fmaf(-0.5f, bf_hi(s23), bf_hi(p23)));
  const float L2E = 1.4426950408889634f;
  const u32 e01 = pk_bf16(__builtin_amdgcn_exp2f(bf_lo(x01) * L2E), __builtin_amdgcn_exp2f(bf_hi(x01) * L2E));
  const u32 e23 = pk_bf16(__builtin_amdgcn_exp2f(bf_lo(x23) * L2E), __builtin_amdgcn_exp2f(bf_hi(x23) * L2E));
  const float R = 0.35355339059327373f;
  const u32 y01 = pk_bf16(bf_lo(e01) * R, bf_hi(e01) * R), y23 = pk_bf16(bf_lo(e23) * R, bf_hi(e23) * R);
  u32x2 o;
  o[0] = pk_bf16(bf_lo(y01) + 1e-6f, bf_hi(y01) + 1e-6f);
  o[1] = pk_bf16(bf_lo(y23) + 1e-6f, bf_hi(y23) + 1e-6f);
  return o;
}

__global__ void __launch_bounds__(NWAVES * 64, 1)
rfa_main(const char* __restrict__ qg, const char* __restrict__ kg, const char* __restrict__ vg, const char* __restrict__ og,
         float* __restrict__ ws) {
  __shared__ __attribute__((aligned(16))) char smem[LDS_BYTES];
  const int lane = threadIdx.x & 63, wave = __builtin_amdgcn_readfirstlane(threadIdx.x >> 6);
  const int g = lane >> 4, c = lane & 15;
  const int bh = blockIdx.x >> 2, split = blockIdx.x & 3;
  const float SCALE = 0.29730177875068026f;
  const u32 lds0 = (u32)(size_t)(LAS char*)smem + wave * WAVE_LDS;
  const u32 aux = lds0 + NSLOT * TILE_B;

  u32 koff[4], voffs[4];
#pragma unroll
  for (int p = 0; p < 4; ++p) {
    const u32 row = 4 * p + g;
    koff[p] = g * 256 + ((c ^ row) << 4);
    voffs[p] = g * 256 + ((c ^ ((row & 7) << 1)) << 4);
  }
  const size_t wg_off = ((size_t)bh * SK + (size_t)split * (SK / NSPLIT)) * (D * 2) + (size_t)wave * TILE_B;
  const char* kbase = kg + wg_off;
  const char* vbase = vg + wg_off;
  constexpr size_t TSTEP = (size_t)NWAVES * TILE_B;

  dma1k(og, lane * 16, aux);
  dma1k(og + 1024, lane * 16, aux + 1024);
  dma1k(qg + bh * (D * 2), c * 16, aux + 2048);
#pragma unroll
  for (int f = 0; f < NSLOT; ++f) fill4k(kbase + f * TSTEP, koff, lds0 + f * TILE_B);

  u32 kra[4], vra[8];
#pragma unroll
  for (int i = 0; i < 4; ++i) kra[i] = lds0 + c * 256 + (((4 * i + g) ^ c) << 4);
  {
    const u32 row = 4 * g + (c >> 2);
#pragma unroll
    for (int b = 0; b < 8; ++b) vra[b] = lds0 + row * 256 + ((b ^ (row & 7)) << 5) + (c & 3) * 8;
  }

  wait_vm<4 * NSLOT>();
  u32x4 omX[4], omY[4];
  f32x4 phq;
  {
    u32x4 om[4], qf[4];
#pragma unroll
    for (int i = 0; i < 4; ++i) {
      om[i] = lds_rd128(aux + (c & 7) * 256 + (4 * i + g) * 16);
      qf[i] = lds_rd128(aux + 2048 + (4 * i + g) * 16);
    }
    f32x4 qp = {0.f, 0.f, 0.f, 0.f};
    float qs = 0.f;
#pragma unroll
    for (int i = 0; i < 4; ++i) {
#pragma unroll
      for (int j = 0; j < 4; ++j) {
        qf[i][j] = scale_pk(qf[i][j], SCALE);
        qs = dot2(qf[i][j], qf[i][j], qs);
        omX[i][j] = c < 8 ? om[i][j] : 0u;
        omY[i][j] = c < 8 ? 0u : om[i][j];
      }
      qp = mfma32(om[i], qf[i], qp);
    }
    f32x4 qq = {0.f, 0.f, 0.f, 0.f};
    qq = mfma4f(1.0f, qs, qq);
    const u32x2 pq = phi4(qp, qq);
    phq[0] = bf_lo(pq[0]); phq[1] = bf_hi(pq[0]); phq[2] = bf_lo(pq[1]); phq[3] = bf_hi(pq[1]);
  }
  const float oneX = c < 8 ? 1.0f : 0.0f, oneY = c < 8 ? 0.0f : 1.0f;
  asm volatile("" : "+v"(phq));
  wait_lgkm0();

#pragma unroll 1
  for (int p = 0; p < TPW / 2; ++p) {
    const u32 so = (u32)((2 * p) & (NSLOT - 1)) * TILE_B;
    const bool nk = p < (TPW - NSLOT) / 2;
    const char* nsrc = nk ? kbase + (size_t)(2 * p + NSLOT) * TSTEP : vbase + (size_t)(2 * p + NSLOT - TPW) * TSTEP;
    u32 no[4];
#pragma unroll
    for (int i = 0; i < 4; ++i) no[i] = nk ? koff[i] : voffs[i];
    u32x4 kx[4], ky[4];
    wait_vm<4 * (NSLOT - 1)>();
#pragma unroll
    for (int i = 0; i < 4; ++i) kx[i] = lds_rd128(kra[i] + so);
    wait_lgkm0();
    fill4k(nsrc, no, lds0 + so);
    wait_vm<4 * (NSLOT - 1)>();
#pragma unroll
    for (int i = 0; i < 4; ++i) ky[i] = lds_rd128(kra[i] + so + TILE_B);
    wait_lgkm0();
    fill4k(nsrc + TSTEP, no, lds0 + so + TILE_B);
    PIN4(kx); PIN4(ky);

    f32x4 pr = {0.f, 0.f, 0.f, 0.f};
    float sx = 0.f, sy = 0.f;
#pragma unroll
    for (int i = 0; i < 4; ++i) {
#pragma unroll
      for (int j = 0; j < 4; ++j) { kx[i][j] = scale_pk(kx[i][j], SCALE); sx = dot2(kx[i][j], kx[i][j], sx); }
      pr = mfma32(kx[i], omX[i], pr);
    }
#pragma unroll
    for (int i = 0; i < 4; ++i) {
#pragma unroll
      for (int j = 0; j < 4; ++j) { ky[i][j] = scale_pk(ky[i][j], SCALE); sy = dot2(ky[i][j], ky[i][j], sy); }
      pr = mfma32(ky[i], omY[i], pr);
    }
    f32x4 sq = {0.f, 0.f, 0.f, 0.f};
    sq = mfma4f(sx, oneX, sq);
    sq = mfma4f(sy, oneY, sq);
    const u32x2 ph = phi4(pr, sq);
    *(LAS u32x2*)(size_t)(aux + p * 512 + lane * 8) = ph;
  }

  f32x4 acc[8], zacc = {0.f, 0.f, 0.f, 0.f};
#pragma unroll
  for (int b = 0; b < 8; ++b) acc[b] = f32x4{0.f, 0.f, 0.f, 0.f};
  const u32x4 ones = {0x3f803f80u, 0x3f803f80u, 0x3f803f80u, 0x3f803f80u};

  auto vstep = [&](int p, u32 so, auto waitx, auto fillx, auto waity, auto filly) {
    const u32x2 ph = *(const LAS u32x2*)(size_t)(aux + p * 512 + lane * 8);
    u32x4 a;
    a[0] = c < 8 ? ph[0] : 0u; a[1] = c < 8 ? ph[1] : 0u; a[2] = c < 8 ? 0u : ph[0]; a[3] = c < 8 ? 0u : ph[1];
    u32x2 vx[8], vy[8];
    waitx();
#pragma unroll
    for (int b = 0; b < 8; ++b) vx[b] = lds_tr(vra[b] + so);
    fillx();
    waity();
#pragma unroll
    for (int b = 0; b < 8; ++b) vy[b] = lds_tr(vra[b] + so + TILE_B);
    filly();
    PIN8(vx); PIN8(vy);
    zacc = mfma32(a, ones, zacc);
#pragma unroll
    for (int b = 0; b < 8; ++b) {
      const u32x4 bb = {vx[b][0], vx[b][1], vy[b][0], vy[b][1]};
      acc[b] = mfma32(a, bb, acc[b]);
    }
  };
#pragma unroll 1
  for (int p = 0; p < (TPW - NSLOT) / 2; ++p) {
    const u32 so = (u32)((2 * p) & (NSLOT - 1)) * TILE_B;
    const char* nsrc = vbase + (size_t)(2 * p + NSLOT) * TSTEP;
    vstep(p, so, [&] { wait_vm<4 * (NSLOT - 1)>(); }, [&] { wait_lgkm0(); fill4k(nsrc, voffs, lds0 + so); },
          [&] { wait_vm<4 * (NSLOT - 1)>(); }, [&] { wait_lgkm0(); fill4k(nsrc + TSTEP, voffs, lds0 + so + TILE_B); });
  }
  auto nop = [] {};
  vstep(4, 0 * TILE_B, [&] { wait_vm<28>(); }, nop, [&] { wait_vm<24>(); }, nop);
  vstep(5, 2 * TILE_B, [&] { wait_vm<20>(); }, nop, [&] { wait_vm<16>(); }, nop);
  vstep(6, 4 * TILE_B, [&] { wait_vm<12>(); }, nop, [&] { wait_vm<8>(); }, nop);
  vstep(7, 6 * TILE_B, [&] { wait_vm<4>(); }, nop, [&] { wait_vm<0>(); }, nop);

  float t[8];
#pragma unroll
  for (int b = 0; b < 8; ++b)
    t[b] = fmaf(phq[3], acc[b][3], fmaf(phq[2], acc[b][2], fmaf(phq[1], acc[b][1], phq[0] * acc[b][0])));
  float dn = fmaf(phq[3], zacc[3], fmaf(phq[2], zacc[2], fmaf(phq[1], zacc[1], phq[0] * zacc[0])));
  float u[4];
#pragma unroll
  for (int b = 0; b < 4; ++b) {
    auto s = __builtin_amdgcn_permlane32_swap(__float_as_uint(t[b]), __float_as_uint(t[b + 4]), false, false);
    u[b] = __uint_as_float(s[0]) + __uint_as_float(s[1]);
  }
  float w2[2];
#pragma unroll
  for (int b = 0; b < 2; ++b) {
    auto s = __builtin_amdgcn_permlane16_swap(__float_as_uint(u[b]), __float_as_uint(u[b + 2]), false, false);
    w2[b] = __uint_as_float(s[0]) + __uint_as_float(s[1]);
  }
  {
    auto s = __builtin_amdgcn_permlane32_swap(__float_as_uint(dn), __float_as_uint(dn), false, false);
    dn = __uint_as_float(s[0]) + __uint_as_float(s[1]);
    auto s2 = __builtin_amdgcn_permlane16_swap(__float_as_uint(dn), __float_as_uint(dn), false, false);
    dn = __uint_as_float(s2[0]) + __uint_as_float(s2[1]);
  }
  LAS float* red = (LAS float*)(size_t)lds0;
  red[32 * g + c] = w2[0];
  red[32 * g + 16 + c] = w2[1];
  if (lane == 0) red[128] = dn;
  __syncthreads();
  if (threadIdx.x < 129) {
    const u32 base = (u32)(size_t)(LAS char*)smem;
    float s = 0.f;
#pragma unroll
    for (int w = 0; w < NWAVES; ++w) s += ((const LAS float*)(size_t)(base + w * WAVE_LDS))[threadIdx.x];
    ws[(size_t)blockIdx.x * PART_F + threadIdx.x] = s;
  }
}

__global__ void __launch_bounds__(128) rfa_combine(const float* __restrict__ ws, __hip_bfloat16* __restrict__ out) {
  const int bh = blockIdx.x, d = threadIdx.x;
  float num = 0.f, den = 0.f;
#pragma unroll
  for (int s = 0; s < NSPLIT; ++s) {
    const float* p = ws + (size_t)(bh * NSPLIT + s) * PART_F;
    num += p[d]; den += p[128];
  }
  num = __bfloat162float(__float2bfloat16(num));
  den = __bfloat162float(__float2bfloat16(den));
  den = __bfloat162float(__float2bfloat16(den + 1e-6f));
  out[bh * D + d] = __float2bfloat16(num / den);
}

extern "C" void kernel_launch(void* const* d_in, const int* in_sizes, int n_in,
                              void* d_out, int out_size, void* d_ws, size_t ws_size,
                              hipStream_t stream) {
  rfa_main<<<NBH * NSPLIT, NWAVES * 64, 0, stream>>>((const char*)d_in[0], (const char*)d_in[1], (const char*)d_in[2],
                                                      (const char*)d_in[3], (float*)d_ws);
  rfa_combine<<<NBH, 128, 0, stream>>>((const float*)d_ws, (__hip_bfloat16*)d_out);
}
```

```cpp
#include <hip/hip_runtime.h>
#include <hip/hip_bf16.h>
#include <stdint.h>

typedef unsigned int u32;
typedef unsigned long long u64;
using u32x2 = __attribute__((ext_vector_type(2))) unsigned;
using u32x4 = __attribute__((ext_vector_type(4))) unsigned;
using f32x2 = __attribute__((ext_vector_type(2))) float;
using f32x4 = __attribute__((ext_vector_type(4))) float;
using bf16x2 = __attribute__((ext_vector_type(2))) __bf16;
using bf16x8 = __attribute__((ext_vector_type(8))) __bf16;
using s16x4 = __attribute__((ext_vector_type(4))) short;
#define LAS __attribute__((address_space(3)))
#define GAS __attribute__((address_space(1)))

constexpr int NBH = 64, SK = 4096, D = 128, M = 8;
constexpr int NSPLIT = 4, NWAVES = 4;
constexpr int TILE_B = 4096;
constexpr int NSLOT = 8;
constexpr int TPW = SK / NSPLIT / 16 / NWAVES;
constexpr int WAVE_LDS = NSLOT * TILE_B + 4096;
constexpr int LDS_BYTES = NWAVES * WAVE_LDS;
constexpr int PART_F = 132;

__device__ __forceinline__ float bf_lo(u32 x) { return __uint_as_float(x << 16); }
__device__ __forceinline__ float bf_hi(u32 x) { return __uint_as_float(x & 0xffff0000u); }
__device__ __forceinline__ u32 pk_bf16(float a, float b) {
  f32x2 f = {a, b};
  return __builtin_bit_cast(u32, __builtin_convertvector(f, bf16x2));
}
__device__ __forceinline__ u32 scale_pk(u32 x, float s) {
  f32x2 f = {bf_lo(x), bf_hi(x)};
  f = f * s;
  return __builtin_bit_cast(u32, __builtin_convertvector(f, bf16x2));
}
__device__ __forceinline__ float dot2(u32 a, u32 b, float c) {
  return __builtin_amdgcn_fdot2_f32_bf16(__builtin_bit_cast(bf16x2, a), __builtin_bit_cast(bf16x2, b), c, false);
}
__device__ __forceinline__ f32x4 mfma32(u32x4 a, u32x4 b, f32x4 c) {
  return __builtin_amdgcn_mfma_f32_16x16x32_bf16(__builtin_bit_cast(bf16x8, a), __builtin_bit_cast(bf16x8, b), c, 0, 0, 0);
}
__device__ __forceinline__ f32x4 mfma4f(float a, float b, f32x4 c) { return __builtin_amdgcn_mfma_f32_16x16x4f32(a, b, c, 0, 0, 0); }

__device__ __forceinline__ void fill4k(const char* sbase, const u32 (&voff)[4], u32 lds) {
  u32 keep;
  asm volatile("s_mov_b32 %0, m0\n\ts_mov_b32 m0, %6\n\ts_nop 0\n\t"
               "global_load_lds_dwordx4 %1, %5 offset:0 sc0 sc1 nt\n\t"
               "global_load_lds_dwordx4 %2, %5 offset:1024 sc0 sc1 nt\n\t"
               "global_load_lds_dwordx4 %3, %5 offset:2048 sc0 sc1 nt\n\t"
               "global_load_lds_dwordx4 %4, %5 offset:3072 sc0 sc1 nt\n\t"
               "s_mov_b32 m0, %0"
               : "=&s"(keep) : "v"(voff[0]), "v"(voff[1]), "v"(voff[2]), "v"(voff[3]), "s"(sbase), "s"(lds) : "memory");
}
__device__ __forceinline__ void dma1k(const char* sbase, u32 voff, u32 lds) {
  u32 keep;
  asm volatile("s_mov_b32 %0, m0\n\ts_mov_b32 m0, %3\n\ts_nop 0\n\t"
               "global_load_lds_dwordx4 %1, %2\n\t"
               "s_mov_b32 m0, %0"
               : "=&s"(keep) : "v"(voff), "s"(sbase), "s"(lds) : "memory");
}
template <int N> __device__ __forceinline__ void wait_vm() { asm volatile("s_waitcnt vmcnt(%0)" :: "i"(N) : "memory"); }
__device__ __forceinline__ void wait_lgkm0() { asm volatile("s_waitcnt lgkmcnt(0)" ::: "memory"); }
#define PIN4(x) asm volatile("" : "+v"(x[0]), "+v"(x[1]), "+v"(x[2]), "+v"(x[3]) :: "memory")
#define PIN8(x) asm volatile("" : "+v"(x[0]), "+v"(x[1]), "+v"(x[2]), "+v"(x[3]), "+v"(x[4]), "+v"(x[5]), "+v"(x[6]), "+v"(x[7]) :: "memory")
__device__ __forceinline__ u32x4 lds_rd128(u32 addr) { return *(const LAS u32x4*)(size_t)addr; }
__device__ __forceinline__ u32x2 lds_tr(u32 addr) {
  return __builtin_bit_cast(u32x2, __builtin_amdgcn_ds_read_tr16_b64_v4i16((LAS s16x4*)(size_t)addr));
}

__device__ __forceinline__ u32x2 phi4(f32x4 pr, f32x4 sq) {
  const u32 p01 = pk_bf16(pr[0], pr[1]), p23 = pk_bf16(pr[2], pr[3]);
  const u32 s01 = pk_bf16(sq[0], sq[1]), s23 = pk_bf16(sq[2], sq[3]);
  const u32 x01 = pk_bf16(fmaf(-0.5f, bf_lo(s01), bf_lo(p01)), fmaf(-0.5f, bf_hi(s01), bf_hi(p01)));
  const u32 x23 = pk_bf16(fmaf(-0.5f, bf_lo(s23), bf_lo(p23)), fmaf(-0.5f, bf_hi(s23), bf_hi(p23)));
  const float L2E = 1.4426950408889634f;
  const u32 e01 = pk_bf16(__builtin_amdgcn_exp2f(bf_lo(x01) * L2E), __builtin_amdgcn_exp2f(bf_hi(x01) * L2E));
  const u32 e23 = pk_bf16(__builtin_amdgcn_exp2f(bf_lo(x23) * L2E), __builtin_amdgcn_exp2f(bf_hi(x23) * L2E));
  const float R = 0.35355339059327373f;
  const u32 y01 = pk_bf16(bf_lo(e01) * R, bf_hi(e01) * R), y23 = pk_bf16(bf_lo(e23) * R, bf_hi(e23) * R);
  u32x2 o;
  o[0] = pk_bf16(bf_lo(y01) + 1e-6f, bf_hi(y01) + 1e-6f);
  o[1] = pk_bf16(bf_lo(y23) + 1e-6f, bf_hi(y23) + 1e-6f);
  return o;
}

#define STAMP(k)
#define STAMPV(k)
__global__ void __launch_bounds__(NWAVES * 64, 1)
rfa_main(const char* __restrict__ qg, const char* __restrict__ kg, const char* __restrict__ vg, const char* __restrict__ og,
         float* __restrict__ ws) {
  __shared__ __attribute__((aligned(16))) char smem[LDS_BYTES];
  const int lane = threadIdx.x & 63, wave = __builtin_amdgcn_readfirstlane(threadIdx.x >> 6);
  const int g = lane >> 4, c = lane & 15;
  STAMPV(0);
  const int bh = blockIdx.x >> 2, split = blockIdx.x & 3;
  const float SCALE = 0.29730177875068026f;
  const u32 lds0 = (u32)(size_t)(LAS char*)smem + wave * WAVE_LDS;
  const u32 aux = lds0 + NSLOT * TILE_B;

  u32 koff[4], voffs[4];
#pragma unroll
  for (int p = 0; p < 4; ++p) {
    const u32 row = 4 * p + g;
    koff[p] = g * 256 + ((c ^ row) << 4);
    voffs[p] = g * 256 + ((c ^ ((row & 7) << 1)) << 4);
  }
  const size_t wg_off = ((size_t)bh * SK + (size_t)split * (SK / NSPLIT)) * (D * 2) + (size_t)wave * TILE_B;
  const char* kbase = kg + wg_off;
  const char* vbase = vg + wg_off;
  constexpr size_t TSTEP = (size_t)NWAVES * TILE_B;

  dma1k(og, lane * 16, aux);
  dma1k(og + 1024, lane * 16, aux + 1024);
  dma1k(qg + bh * (D * 2), c * 16, aux + 2048);
#pragma unroll
  for (int f = 0; f < NSLOT; ++f) fill4k(kbase + f * TSTEP, koff, lds0 + f * TILE_B);

  STAMPV(1);
  u32 kra[4], vra[8];
#pragma unroll
  for (int i = 0; i < 4; ++i) kra[i] = lds0 + c * 256 + (((4 * i + g) ^ c) << 4);
  {
    const u32 row = 4 * g + (c >> 2);
#pragma unroll
    for (int b = 0; b < 8; ++b) vra[b] = lds0 + row * 256 + ((b ^ (row & 7)) << 5) + (c & 3) * 8;
  }

  wait_vm<4 * NSLOT>();
  u32x4 omX[4], omY[4];
  f32x4 phq;
  {
    u32x4 om[4], qf[4];
#pragma unroll
    for (int i = 0; i < 4; ++i) {
      om[i] = lds_rd128(aux + (c & 7) * 256 + (4 * i + g) * 16);
      qf[i] = lds_rd128(aux + 2048 + (4 * i + g) * 16);
    }
    f32x4 qp = {0.f, 0.f, 0.f, 0.f};
    float qs = 0.f, qs2 = 0.f;
#pragma unroll
    for (int i = 0; i < 4; ++i) {
#pragma unroll
      for (int j = 0; j < 4; ++j) {
        qf[i][j] = scale_pk(qf[i][j], SCALE);
        if (j & 1) qs2 = dot2(qf[i][j], qf[i][j], qs2); else qs = dot2(qf[i][j], qf[i][j], qs);
        omX[i][j] = c < 8 ? om[i][j] : 0u;
        omY[i][j] = c < 8 ? 0u : om[i][j];
      }
      qp = mfma32(om[i], qf[i], qp);
    }
    f32x4 qq = {0.f, 0.f, 0.f, 0.f};
    qq = mfma4f(1.0f, qs + qs2, qq);
    const u32x2 pq = phi4(qp, qq);
    phq[0] = bf_lo(pq[0]); phq[1] = bf_hi(pq[0]); phq[2] = bf_lo(pq[1]); phq[3] = bf_hi(pq[1]);
  }
  const float oneX = c < 8 ? 1.0f : 0.0f, oneY = c < 8 ? 0.0f : 1.0f;
  asm volatile("" : "+v"(phq));
  wait_lgkm0();

  STAMPV(2);
#pragma unroll 1
  for (int p = 0; p < TPW / 2; ++p) {
    const u32 so = (u32)((2 * p) & (NSLOT - 1)) * TILE_B;
    const bool nk = p < (TPW - NSLOT) / 2;
    const char* nsrc = nk ? kbase + (size_t)(2 * p + NSLOT) * TSTEP : vbase + (size_t)(2 * p + NSLOT - TPW) * TSTEP;
    u32 no[4];
#pragma unroll
    for (int i = 0; i < 4; ++i) no[i] = nk ? koff[i] : voffs[i];
    u32x4 kx[4], ky[4];
    wait_vm<4 * (NSLOT - 1)>();
#pragma unroll
    for (int i = 0; i < 4; ++i) kx[i] = lds_rd128(kra[i] + so);
    wait_lgkm0();
    fill4k(nsrc, no, lds0 + so);
    wait_vm<4 * (NSLOT - 1)>();
#pragma unroll
    for (int i = 0; i < 4; ++i) ky[i] = lds_rd128(kra[i] + so + TILE_B);
    wait_lgkm0();
    fill4k(nsrc + TSTEP, no, lds0 + so + TILE_B);
    PIN4(kx); PIN4(ky);

    f32x4 pr = {0.f, 0.f, 0.f, 0.f};
    float sx = 0.f, sy = 0.f, sx2 = 0.f, sy2 = 0.f;
#pragma unroll
    for (int i = 0; i < 4; ++i) {
#pragma unroll
      for (int j = 0; j < 4; ++j) { kx[i][j] = scale_pk(kx[i][j], SCALE); if (j & 1) sx2 = dot2(kx[i][j], kx[i][j], sx2); else sx = dot2(kx[i][j], kx[i][j], sx); }
      pr = mfma32(kx[i], omX[i], pr);
    }
#pragma unroll
    for (int i = 0; i < 4; ++i) {
#pragma unroll
      for (int j = 0; j < 4; ++j) { ky[i][j] = scale_pk(ky[i][j], SCALE); if (j & 1) sy2 = dot2(ky[i][j], ky[i][j], sy2); else sy = dot2(ky[i][j], ky[i][j], sy); }
      pr = mfma32(ky[i], omY[i], pr);
    }
    f32x4 sq = {0.f, 0.f, 0.f, 0.f};
    sq = mfma4f(sx + sx2, oneX, sq);
    sq = mfma4f(sy + sy2, oneY, sq);
    const u32x2 ph = phi4(pr, sq);
    *(LAS u32x2*)(size_t)(aux + p * 512 + lane * 8) = ph;
  }

  STAMPV(3);
  f32x4 acc[8], zacc = {0.f, 0.f, 0.f, 0.f};
#pragma unroll
  for (int b = 0; b < 8; ++b) acc[b] = f32x4{0.f, 0.f, 0.f, 0.f};
  const u32x4 ones = {0x3f803f80u, 0x3f803f80u, 0x3f803f80u, 0x3f803f80u};

  auto vstep = [&](int p, u32 so, auto waitx, auto fillx, auto waity, auto filly) {
    const u32x2 ph = *(const LAS u32x2*)(size_t)(aux + p * 512 + lane * 8);
    u32x4 a;
    a[0] = c < 8 ? ph[0] : 0u; a[1] = c < 8 ? ph[1] : 0u; a[2] = c < 8 ? 0u : ph[0]; a[3] = c < 8 ? 0u : ph[1];
    u32x2 vx[8], vy[8];
    waitx();
#pragma unroll
    for (int b = 0; b < 8; ++b) vx[b] = lds_tr(vra[b] + so);
    fillx();
    waity();
#pragma unroll
    for (int b = 0; b < 8; ++b) vy[b] = lds_tr(vra[b] + so + TILE_B);
    filly();
    PIN8(vx); PIN8(vy);
    zacc = mfma32(a, ones, zacc);
#pragma unroll
    for (int b = 0; b < 8; ++b) {
      const u32x4 bb = {vx[b][0], vx[b][1], vy[b][0], vy[b][1]};
      acc[b] = mfma32(a, bb, acc[b]);
    }
  };
#pragma unroll 1
  for (int p = 0; p < (TPW - NSLOT) / 2; ++p) {
    const u32 so = (u32)((2 * p) & (NSLOT - 1)) * TILE_B;
    const char* nsrc = vbase + (size_t)(2 * p + NSLOT) * TSTEP;
    vstep(p, so, [&] { wait_vm<4 * (NSLOT - 1)>(); }, [&] { wait_lgkm0(); fill4k(nsrc, voffs, lds0 + so); },
          [&] { wait_vm<4 * (NSLOT - 1)>(); }, [&] { wait_lgkm0(); fill4k(nsrc + TSTEP, voffs, lds0 + so + TILE_B); });
  }
  auto nop = [] {};
  vstep(4, 0 * TILE_B, [&] { wait_vm<28>(); }, nop, [&] { wait_vm<24>(); }, nop);
  vstep(5, 2 * TILE_B, [&] { wait_vm<20>(); }, nop, [&] { wait_vm<16>(); }, nop);
  vstep(6, 4 * TILE_B, [&] { wait_vm<12>(); }, nop, [&] { wait_vm<8>(); }, nop);
  vstep(7, 6 * TILE_B, [&] { wait_vm<4>(); }, nop, [&] { wait_vm<0>(); }, nop);

  STAMPV(4);
  float t[8];
#pragma unroll
  for (int b = 0; b < 8; ++b)
    t[b] = fmaf(phq[3], acc[b][3], fmaf(phq[2], acc[b][2], fmaf(phq[1], acc[b][1], phq[0] * acc[b][0])));
  float dn = fmaf(phq[3], zacc[3], fmaf(phq[2], zacc[2], fmaf(phq[1], zacc[1], phq[0] * zacc[0])));
  float u[4];
#pragma unroll
  for (int b = 0; b < 4; ++b) {
    auto s = __builtin_amdgcn_permlane32_swap(__float_as_uint(t[b]), __float_as_uint(t[b + 4]), false, false);
    u[b] = __uint_as_float(s[0]) + __uint_as_float(s[1]);
  }
  float w2[2];
#pragma unroll
  for (int b = 0; b < 2; ++b) {
    auto s = __builtin_amdgcn_permlane16_swap(__float_as_uint(u[b]), __float_as_uint(u[b + 2]), false, false);
    w2[b] = __uint_as_float(s[0]) + __uint_as_float(s[1]);
  }
  {
    auto s = __builtin_amdgcn_permlane32_swap(__float_as_uint(dn), __float_as_uint(dn), false, false);
    dn = __uint_as_float(s[0]) + __uint_as_float(s[1]);
    auto s2 = __builtin_amdgcn_permlane16_swap(__float_as_uint(dn), __float_as_uint(dn), false, false);
    dn = __uint_as_float(s2[0]) + __uint_as_float(s2[1]);
  }
  LAS float* red = (LAS float*)(size_t)lds0;
  red[32 * g + c] = w2[0];
  red[32 * g + 16 + c] = w2[1];
  if (lane == 0) red[128] = dn;
  __syncthreads();
  if (threadIdx.x < 129) {
    const u32 base = (u32)(size_t)(LAS char*)smem;
    float s = 0.f;
#pragma unroll
    for (int w = 0; w < NWAVES; ++w) s += ((const LAS float*)(size_t)(base + w * WAVE_LDS))[threadIdx.x];
    ws[(size_t)blockIdx.x * PART_F + threadIdx.x] = s;
  }
}

__global__ void __launch_bounds__(128) rfa_combine(const float* __restrict__ ws, __hip_bfloat16* __restrict__ out) {
  const int bh = blockIdx.x, d = threadIdx.x;
  float num = 0.f, den = 0.f;
#pragma unroll
  for (int s = 0; s < NSPLIT; ++s) {
    const float* p = ws + (size_t)(bh * NSPLIT + s) * PART_F;
    num += p[d]; den += p[128];
  }
  num = __bfloat162float(__float2bfloat16(num));
  den = __bfloat162float(__float2bfloat16(den));
  den = __bfloat162float(__float2bfloat16(den + 1e-6f));
  out[bh * D + d] = __float2bfloat16(num / den);
}

extern "C" void kernel_launch(void* const* d_in, const int* in_sizes, int n_in,
                              void* d_out, int out_size, void* d_ws, size_t ws_size,
                              hipStream_t stream) {
  rfa_main<<<NBH * NSPLIT, NWAVES * 64, 0, stream>>>((const char*)d_in[0], (const char*)d_in[1], (const char*)d_in[2],
                                                      (const char*)d_in[3], (float*)d_ws);
  rfa_combine<<<NBH, 128, 0, stream>>>((const float*)d_ws, (__hip_bfloat16*)d_out);
}
```

```cpp
#include <hip/hip_runtime.h>
#include <hip/hip_bf16.h>
#include <stdint.h>

typedef unsigned int u32;
typedef unsigned long long u64;
using u32x2 = __attribute__((ext_vector_type(2))) unsigned;
using u32x4 = __attribute__((ext_vector_type(4))) unsigned;
using f32x2 = __attribute__((ext_vector_type(2))) float;
using f32x4 = __attribute__((ext_vector_type(4))) float;
using bf16x2 = __attribute__((ext_vector_type(2))) __bf16;
using bf16x8 = __attribute__((ext_vector_type(8))) __bf16;
using s16x4 = __attribute__((ext_vector_type(4))) short;
#define LAS __attribute__((address_space(3)))
#define GAS __attribute__((address_space(1)))

constexpr int NBH = 64, SK = 4096, D = 128, M = 8;
constexpr int NSPLIT = 4, NWAVES = 4;
constexpr int TILE_B = 4096;
constexpr int NSLOT = 8;
constexpr int TPW = SK / NSPLIT / 16 / NWAVES;
constexpr int WAVE_LDS = NSLOT * TILE_B + 4096;
constexpr int LDS_BYTES = NWAVES * WAVE_LDS;
constexpr int PART_F = 132;

__device__ __forceinline__ float bf_lo(u32 x) { return __uint_as_float(x << 16); }
__device__ __forceinline__ float bf_hi(u32 x) { return __uint_as_float(x & 0xffff0000u); }
__device__ __forceinline__ u32 pk_bf16(float a, float b) {
  f32x2 f = {a, b};
  return __builtin_bit_cast(u32, __builtin_convertvector(f, bf16x2));
}
__device__ __forceinline__ u32 scale_pk(u32 x, float s) {
  f32x2 f = {bf_lo(x), bf_hi(x)};
  f = f * s;
  return __builtin_bit_cast(u32, __builtin_convertvector(f, bf16x2));
}
__device__ __forceinline__ float dot2(u32 a, u32 b, float c) {
  return __builtin_amdgcn_fdot2_f32_bf16(__builtin_bit_cast(bf16x2, a), __builtin_bit_cast(bf16x2, b), c, false);
}
__device__ __forceinline__ f32x4 mfma32(u32x4 a, u32x4 b, f32x4 c) {
  return __builtin_amdgcn_mfma_f32_16x16x32_bf16(__builtin_bit_cast(bf16x8, a), __builtin_bit_cast(bf16x8, b), c, 0, 0, 0);
}
__device__ __forceinline__ f32x4 mfma4f(float a, float b, f32x4 c) { return __builtin_amdgcn_mfma_f32_16x16x4f32(a, b, c, 0, 0, 0); }

__device__ __forceinline__ void fill4k(const char* sbase, const u32 (&voff)[4], u32 lds) {
  u32 keep;
  asm volatile("s_mov_b32 %0, m0\n\ts_mov_b32 m0, %6\n\ts_nop 0\n\t"
               "global_load_lds_dwordx4 %1, %5 offset:0 nt\n\t"
               "global_load_lds_dwordx4 %2, %5 offset:1024 nt\n\t"
               "global_load_lds_dwordx4 %3, %5 offset:2048 nt\n\t"
               "global_load_lds_dwordx4 %4, %5 offset:3072 nt\n\t"
               "s_mov_b32 m0, %0"
               : "=&s"(keep) : "v"(voff[0]), "v"(voff[1]), "v"(voff[2]), "v"(voff[3]), "s"(sbase), "s"(lds) : "memory");
}
__device__ __forceinline__ void dma1k(const char* sbase, u32 voff, u32 lds) {
  u32 keep;
  asm volatile("s_mov_b32 %0, m0\n\ts_mov_b32 m0, %3\n\ts_nop 0\n\t"
               "global_load_lds_dwordx4 %1, %2\n\t"
               "s_mov_b32 m0, %0"
               : "=&s"(keep) : "v"(voff), "s"(sbase), "s"(lds) : "memory");
}
template <int N> __device__ __forceinline__ void wait_vm() { asm volatile("s_waitcnt vmcnt(%0)" :: "i"(N) : "memory"); }
__device__ __forceinline__ void wait_lgkm0() { asm volatile("s_waitcnt lgkmcnt(0)" ::: "memory"); }
#define PIN4(x) asm volatile("" : "+v"(x[0]), "+v"(x[1]), "+v"(x[2]), "+v"(x[3]) :: "memory")
#define PIN8(x) asm volatile("" : "+v"(x[0]), "+v"(x[1]), "+v"(x[2]), "+v"(x[3]), "+v"(x[4]), "+v"(x[5]), "+v"(x[6]), "+v"(x[7]) :: "memory")
__device__ __forceinline__ u32x4 lds_rd128(u32 addr) { return *(const LAS u32x4*)(size_t)addr; }
__device__ __forceinline__ u32x2 lds_tr(u32 addr) {
  return __builtin_bit_cast(u32x2, __builtin_amdgcn_ds_read_tr16_b64_v4i16((LAS s16x4*)(size_t)addr));
}

__device__ __forceinline__ u32x2 phi4(f32x4 pr, f32x4 sq) {
  const u32 p01 = pk_bf16(pr[0], pr[1]), p23 = pk_bf16(pr[2], pr[3]);
  const u32 s01 = pk_bf16(sq[0], sq[1]), s23 = pk_bf16(sq[2], sq[3]);
  const u32 x01 = pk_bf16(fmaf(-0.5f, bf_lo(s01), bf_lo(p01)), fmaf(-0.5f, bf_hi(s01), bf_hi(p01)));
  const u32 x23 = pk_bf16(fmaf(-0.5f, bf_lo(s23), bf_lo(p23)), fmaf(-0.5f, bf_hi(s23), bf_hi(p23)));
  const float L2E = 1.4426950408889634f;
  const u32 e01 = pk_bf16(__builtin_amdgcn_exp2f(bf_lo(x01) * L2E), __builtin_amdgcn_exp2f(bf_hi(x01) * L2E));
  const u32 e23 = pk_bf16(__builtin_amdgcn_exp2f(bf_lo(x23) * L2E), __builtin_amdgcn_exp2f(bf_hi(x23) * L2E));
  const float R = 0.35355339059327373f;
  const u32 y01 = pk_bf16(bf_lo(e01) * R, bf_hi(e01) * R), y23 = pk_bf16(bf_lo(e23) * R, bf_hi(e23) * R);
  u32x2 o;
  o[0] = pk_bf16(bf_lo(y01) + 1e-6f, bf_hi(y01) + 1e-6f);
  o[1] = pk_bf16(bf_lo(y23) + 1e-6f, bf_hi(y23) + 1e-6f);
  return o;
}

#define STAMP(k)
#define STAMPV(k)
__global__ void __launch_bounds__(NWAVES * 64, 1)
rfa_main(const char* __restrict__ qg, const char* __restrict__ kg, const char* __restrict__ vg, const char* __restrict__ og,
         float* __restrict__ ws) {
  __shared__ __attribute__((aligned(16))) char smem[LDS_BYTES];
  const int lane = threadIdx.x & 63, wave = __builtin_amdgcn_readfirstlane(threadIdx.x >> 6);
  const int g = lane >> 4, c = lane & 15;
  STAMPV(0);
  const int bh = blockIdx.x >> 2, split = blockIdx.x & 3;
  const float SCALE = 0.29730177875068026f;
  const u32 lds0 = (u32)(size_t)(LAS char*)smem + wave * WAVE_LDS;
  const u32 aux = lds0 + NSLOT * TILE_B;

  u32 koff[4], voffs[4];
#pragma unroll
  for (int p = 0; p < 4; ++p) {
    const u32 row = 4 * p + g;
    koff[p] = g * 256 + ((c ^ row) << 4);
    voffs[p] = g * 256 + ((c ^ ((row & 7) << 1)) << 4);
  }
  const size_t wg_off = ((size_t)bh * SK + (size_t)split * (SK / NSPLIT)) * (D * 2) + (size_t)wave * TILE_B;
  const char* kbase = kg + wg_off;
  const char* vbase = vg + wg_off;
  constexpr size_t TSTEP = (size_t)NWAVES * TILE_B;

  dma1k(og, lane * 16, aux);
  dma1k(og + 1024, lane * 16, aux + 1024);
  dma1k(qg + bh * (D * 2), c * 16, aux + 2048);
#pragma unroll
  for (int f = 0; f < NSLOT; ++f) fill4k(kbase + f * TSTEP, koff, lds0 + f * TILE_B);

  STAMPV(1);
  u32 kra[4], vra[8];
#pragma unroll
  for (int i = 0; i < 4; ++i) kra[i] = lds0 + c * 256 + (((4 * i + g) ^ c) << 4);
  {
    const u32 row = 4 * g + (c >> 2);
#pragma unroll
    for (int b = 0; b < 8; ++b) vra[b] = lds0 + row * 256 + ((b ^ (row & 7)) << 5) + (c & 3) * 8;
  }

  wait_vm<4 * NSLOT>();
  u32x4 omX[4], omY[4];
  f32x4 phq;
  {
    u32x4 om[4], qf[4];
#pragma unroll
    for (int i = 0; i < 4; ++i) {
      om[i] = lds_rd128(aux + (c & 7) * 256 + (4 * i + g) * 16);
      qf[i] = lds_rd128(aux + 2048 + (4 * i + g) * 16);
    }
    f32x4 qp = {0.f, 0.f, 0.f, 0.f};
    float qs = 0.f, qs2 = 0.f;
#pragma unroll
    for (int i = 0; i < 4; ++i) {
#pragma unroll
      for (int j = 0; j < 4; ++j) {
        qf[i][j] = scale_pk(qf[i][j], SCALE);
        if (j & 1) qs2 = dot2(qf[i][j], qf[i][j], qs2); else qs = dot2(qf[i][j], qf[i][j], qs);
        omX[i][j] = c < 8 ? om[i][j] : 0u;
        omY[i][j] = c < 8 ? 0u : om[i][j];
      }
      qp = mfma32(om[i], qf[i], qp);
    }
    f32x4 qq = {0.f, 0.f, 0.f, 0.f};
    qq = mfma4f(1.0f, qs + qs2, qq);
    const u32x2 pq = phi4(qp, qq);
    phq[0] = bf_lo(pq[0]); phq[1] = bf_hi(pq[0]); phq[2] = bf_lo(pq[1]); phq[3] = bf_hi(pq[1]);
  }
  const float oneX = c < 8 ? 1.0f : 0.0f, oneY = c < 8 ? 0.0f : 1.0f;
  asm volatile("" : "+v"(phq));
  wait_lgkm0();

  STAMPV(2);
#pragma unroll 1
  for (int p = 0; p < TPW / 2; ++p) {
    const u32 so = (u32)((2 * p) & (NSLOT - 1)) * TILE_B;
    const bool nk = p < (TPW - NSLOT) / 2;
    const char* nsrc = nk ? kbase + (size_t)(2 * p + NSLOT) * TSTEP : vbase + (size_t)(2 * p + NSLOT - TPW) * TSTEP;
    u32 no[4];
#pragma unroll
    for (int i = 0; i < 4; ++i) no[i] = nk ? koff[i] : voffs[i];
    u32x4 kx[4], ky[4];
    wait_vm<4 * (NSLOT - 1)>();
#pragma unroll
    for (int i = 0; i < 4; ++i) kx[i] = lds_rd128(kra[i] + so);
    wait_lgkm0();
    fill4k(nsrc, no, lds0 + so);
    wait_vm<4 * (NSLOT - 1)>();
#pragma unroll
    for (int i = 0; i < 4; ++i) ky[i] = lds_rd128(kra[i] + so + TILE_B);
    wait_lgkm0();
    fill4k(nsrc + TSTEP, no, lds0 + so + TILE_B);
    PIN4(kx); PIN4(ky);

    f32x4 pr = {0.f, 0.f, 0.f, 0.f};
    float sx = 0.f, sy = 0.f, sx2 = 0.f, sy2 = 0.f;
#pragma unroll
    for (int i = 0; i < 4; ++i) {
#pragma unroll
      for (int j = 0; j < 4; ++j) { kx[i][j] = scale_pk(kx[i][j], SCALE); if (j & 1) sx2 = dot2(kx[i][j], kx[i][j], sx2); else sx = dot2(kx[i][j], kx[i][j], sx); }
      pr = mfma32(kx[i], omX[i], pr);
    }
#pragma unroll
    for (int i = 0; i < 4; ++i) {
#pragma unroll
      for (int j = 0; j < 4; ++j) { ky[i][j] = scale_pk(ky[i][j], SCALE); if (j & 1) sy2 = dot2(ky[i][j], ky[i][j], sy2); else sy = dot2(ky[i][j], ky[i][j], sy); }
      pr = mfma32(ky[i], omY[i], pr);
    }
    f32x4 sq = {0.f, 0.f, 0.f, 0.f};
    sq = mfma4f(sx + sx2, oneX, sq);
    sq = mfma4f(sy + sy2, oneY, sq);
    const u32x2 ph = phi4(pr, sq);
    *(LAS u32x2*)(size_t)(aux + p * 512 + lane * 8) = ph;
  }

  STAMPV(3);
  f32x4 acc[8], zacc = {0.f, 0.f, 0.f, 0.f};
#pragma unroll
  for (int b = 0; b < 8; ++b) acc[b] = f32x4{0.f, 0.f, 0.f, 0.f};
  const u32x4 ones = {0x3f803f80u, 0x3f803f80u, 0x3f803f80u, 0x3f803f80u};

  auto vstep = [&](int p, u32 so, auto waitx, auto fillx, auto waity, auto filly) {
    const u32x2 ph = *(const LAS u32x2*)(size_t)(aux + p * 512 + lane * 8);
    u32x4 a;
    a[0] = c < 8 ? ph[0] : 0u; a[1] = c < 8 ? ph[1] : 0u; a[2] = c < 8 ? 0u : ph[0]; a[3] = c < 8 ? 0u : ph[1];
    u32x2 vx[8], vy[8];
    waitx();
#pragma unroll
    for (int b = 0; b < 8; ++b) vx[b] = lds_tr(vra[b] + so);
    fillx();
    waity();
#pragma unroll
    for (int b = 0; b < 8; ++b) vy[b] = lds_tr(vra[b] + so + TILE_B);
    filly();
    PIN8(vx); PIN8(vy);
    zacc = mfma32(a, ones, zacc);
#pragma unroll
    for (int b = 0; b < 8; ++b) {
      const u32x4 bb = {vx[b][0], vx[b][1], vy[b][0], vy[b][1]};
      acc[b] = mfma32(a, bb, acc[b]);
    }
  };
#pragma unroll 1
  for (int p = 0; p < (TPW - NSLOT) / 2; ++p) {
    const u32 so = (u32)((2 * p) & (NSLOT - 1)) * TILE_B;
    const char* nsrc = vbase + (size_t)(2 * p + NSLOT) * TSTEP;
    vstep(p, so, [&] { wait_vm<4 * (NSLOT - 1)>(); }, [&] { wait_lgkm0(); fill4k(nsrc, voffs, lds0 + so); },
          [&] { wait_vm<4 * (NSLOT - 1)>(); }, [&] { wait_lgkm0(); fill4k(nsrc + TSTEP, voffs, lds0 + so + TILE_B); });
  }
  auto nop = [] {};
  vstep(4, 0 * TILE_B, [&] { wait_vm<28>(); }, nop, [&] { wait_vm<24>(); }, nop);
  vstep(5, 2 * TILE_B, [&] { wait_vm<20>(); }, nop, [&] { wait_vm<16>(); }, nop);
  vstep(6, 4 * TILE_B, [&] { wait_vm<12>(); }, nop, [&] { wait_vm<8>(); }, nop);
  vstep(7, 6 * TILE_B, [&] { wait_vm<4>(); }, nop, [&] { wait_vm<0>(); }, nop);

  STAMPV(4);
  float t[8];
#pragma unroll
  for (int b = 0; b < 8; ++b)
    t[b] = fmaf(phq[3], acc[b][3], fmaf(phq[2], acc[b][2], fmaf(phq[1], acc[b][1], phq[0] * acc[b][0])));
  float dn = fmaf(phq[3], zacc[3], fmaf(phq[2], zacc[2], fmaf(phq[1], zacc[1], phq[0] * zacc[0])));
  float u[4];
#pragma unroll
  for (int b = 0; b < 4; ++b) {
    auto s = __builtin_amdgcn_permlane32_swap(__float_as_uint(t[b]), __float_as_uint(t[b + 4]), false, false);
    u[b] = __uint_as_float(s[0]) + __uint_as_float(s[1]);
  }
  float w2[2];
#pragma unroll
  for (int b = 0; b < 2; ++b) {
    auto s = __builtin_amdgcn_permlane16_swap(__float_as_uint(u[b]), __float_as_uint(u[b + 2]), false, false);
    w2[b] = __uint_as_float(s[0]) + __uint_as_float(s[1]);
  }
  {
    auto s = __builtin_amdgcn_permlane32_swap(__float_as_uint(dn), __float_as_uint(dn), false, false);
    dn = __uint_as_float(s[0]) + __uint_as_float(s[1]);
    auto s2 = __builtin_amdgcn_permlane16_swap(__float_as_uint(dn), __float_as_uint(dn), false, false);
    dn = __uint_as_float(s2[0]) + __uint_as_float(s2[1]);
  }
  LAS float* red = (LAS float*)(size_t)lds0;
  red[32 * g + c] = w2[0];
  red[32 * g + 16 + c] = w2[1];
  if (lane == 0) red[128] = dn;
  __syncthreads();
  if (threadIdx.x < 129) {
    const u32 base = (u32)(size_t)(LAS char*)smem;
    float s = 0.f;
#pragma unroll
    for (int w = 0; w < NWAVES; ++w) s += ((const LAS float*)(size_t)(base + w * WAVE_LDS))[threadIdx.x];
    ws[(size_t)blockIdx.x * PART_F + threadIdx.x] = s;
  }
}

__global__ void __launch_bounds__(128) rfa_combine(const float* __restrict__ ws, __hip_bfloat16* __restrict__ out) {
  const int bh = blockIdx.x, d = threadIdx.x;
  float num = 0.f, den = 0.f;
#pragma unroll
  for (int s = 0; s < NSPLIT; ++s) {
    const float* p = ws + (size_t)(bh * NSPLIT + s) * PART_F;
    num += p[d]; den += p[128];
  }
  num = __bfloat162float(__float2bfloat16(num));
  den = __bfloat162float(__float2bfloat16(den));
  den = __bfloat162float(__float2bfloat16(den + 1e-6f));
  out[bh * D + d] = __float2bfloat16(num / den);
}

extern "C" void kernel_launch(void* const* d_in, const int* in_sizes, int n_in,
                              void* d_out, int out_size, void* d_ws, size_t ws_size,
                              hipStream_t stream) {
  rfa_main<<<NBH * NSPLIT, NWAVES * 64, 0, stream>>>((const char*)d_in[0], (const char*)d_in[1], (const char*)d_in[2],
                                                      (const char*)d_in[3], (float*)d_ws);
  rfa_combine<<<NBH, 128, 0, stream>>>((const float*)d_ws, (__hip_bfloat16*)d_out);
}
```

```cpp
#include <hip/hip_runtime.h>
#include <hip/hip_bf16.h>
#include <stdint.h>

#define XSWZ 0
#define VTOUCH 0
typedef unsigned int u32;
typedef unsigned long long u64;
using u32x2 = __attribute__((ext_vector_type(2))) unsigned;
using u32x4 = __attribute__((ext_vector_type(4))) unsigned;
using f32x2 = __attribute__((ext_vector_type(2))) float;
using f32x4 = __attribute__((ext_vector_type(4))) float;
using bf16x2 = __attribute__((ext_vector_type(2))) __bf16;
using bf16x8 = __attribute__((ext_vector_type(8))) __bf16;
using s16x4 = __attribute__((ext_vector_type(4))) short;
#define LAS __attribute__((address_space(3)))
#define GAS __attribute__((address_space(1)))

constexpr int NBH = 64, SK = 4096, D = 128, M = 8;
constexpr int NSPLIT = 4, NWAVES = 4;
constexpr int TILE_B = 4096;
constexpr int NSLOT = 8;
constexpr int TPW = SK / NSPLIT / 16 / NWAVES;
constexpr int WAVE_LDS = NSLOT * TILE_B + 4096;
constexpr int LDS_BYTES = NWAVES * WAVE_LDS;
constexpr int PART_F = 132;

__device__ __forceinline__ float bf_lo(u32 x) { return __uint_as_float(x << 16); }
__device__ __forceinline__ float bf_hi(u32 x) { return __uint_as_float(x & 0xffff0000u); }
__device__ __forceinline__ u32 pk_bf16(float a, float b) {
  f32x2 f = {a, b};
  return __builtin_bit_cast(u32, __builtin_convertvector(f, bf16x2));
}
__device__ __forceinline__ u32 scale_pk(u32 x, float s) {
  f32x2 f = {bf_lo(x), bf_hi(x)};
  f = f * s;
  return __builtin_bit_cast(u32, __builtin_convertvector(f, bf16x2));
}
__device__ __forceinline__ float dot2(u32 a, u32 b, float c) {
  return __builtin_amdgcn_fdot2_f32_bf16(__builtin_bit_cast(bf16x2, a), __builtin_bit_cast(bf16x2, b), c, false);
}
__device__ __forceinline__ f32x4 mfma32(u32x4 a, u32x4 b, f32x4 c) {
  return __builtin_amdgcn_mfma_f32_16x16x32_bf16(__builtin_bit_cast(bf16x8, a), __builtin_bit_cast(bf16x8, b), c, 0, 0, 0);
}
__device__ __forceinline__ f32x4 mfma4f(float a, float b, f32x4 c) { return __builtin_amdgcn_mfma_f32_16x16x4f32(a, b, c, 0, 0, 0); }

__device__ __forceinline__ void fill4k(const char* sbase, const u32 (&voff)[4], u32 lds) {
  u32 keep;
  asm volatile("s_mov_b32 %0, m0\n\ts_mov_b32 m0, %6\n\ts_nop 0\n\t"
               "global_load_lds_dwordx4 %1, %5 offset:0 nt\n\t"
               "global_load_lds_dwordx4 %2, %5 offset:1024 nt\n\t"
               "global_load_lds_dwordx4 %3, %5 offset:2048 nt\n\t"
               "global_load_lds_dwordx4 %4, %5 offset:3072 nt\n\t"
               "s_mov_b32 m0, %0"
               : "=&s"(keep) : "v"(voff[0]), "v"(voff[1]), "v"(voff[2]), "v"(voff[3]), "s"(sbase), "s"(lds) : "memory");
}
__device__ __forceinline__ void dma1k(const char* sbase, u32 voff, u32 lds) {
  u32 keep;
  asm volatile("s_mov_b32 %0, m0\n\ts_mov_b32 m0, %3\n\ts_nop 0\n\t"
               "global_load_lds_dwordx4 %1, %2\n\t"
               "s_mov_b32 m0, %0"
               : "=&s"(keep) : "v"(voff), "s"(sbase), "s"(lds) : "memory");
}
template <int N> __device__ __forceinline__ void wait_vm() { asm volatile("s_waitcnt vmcnt(%0)" :: "i"(N) : "memory"); }
__device__ __forceinline__ void wait_lgkm0() { asm volatile("s_waitcnt lgkmcnt(0)" ::: "memory"); }
#define PIN4(x) asm volatile("" : "+v"(x[0]), "+v"(x[1]), "+v"(x[2]), "+v"(x[3]) :: "memory")
#define PIN8(x) asm volatile("" : "+v"(x[0]), "+v"(x[1]), "+v"(x[2]), "+v"(x[3]), "+v"(x[4]), "+v"(x[5]), "+v"(x[6]), "+v"(x[7]) :: "memory")
__device__ __forceinline__ u32x4 lds_rd128(u32 addr) { return *(const LAS u32x4*)(size_t)addr; }
__device__ __forceinline__ u32x2 lds_tr(u32 addr) {
  return __builtin_bit_cast(u32x2, __builtin_amdgcn_ds_read_tr16_b64_v4i16((LAS s16x4*)(size_t)addr));
}

__device__ __forceinline__ u32x2 phi4(f32x4 pr, f32x4 sq) {
  const u32 p01 = pk_bf16(pr[0], pr[1]), p23 = pk_bf16(pr[2], pr[3]);
  const u32 s01 = pk_bf16(sq[0], sq[1]), s23 = pk_bf16(sq[2], sq[3]);
  const u32 x01 = pk_bf16(fmaf(-0.5f, bf_lo(s01), bf_lo(p01)), fmaf(-0.5f, bf_hi(s01), bf_hi(p01)));
  const u32 x23 = pk_bf16(fmaf(-0.5f, bf_lo(s23), bf_lo(p23)), fmaf(-0.5f, bf_hi(s23), bf_hi(p23)));
  const float L2E = 1.4426950408889634f;
  const u32 e01 = pk_bf16(__builtin_amdgcn_exp2f(bf_lo(x01) * L2E), __builtin_amdgcn_exp2f(bf_hi(x01) * L2E));
  const u32 e23 = pk_bf16(__builtin_amdgcn_exp2f(bf_lo(x23) * L2E), __builtin_amdgcn_exp2f(bf_hi(x23) * L2E));
  const float R = 0.35355339059327373f;
  const u32 y01 = pk_bf16(bf_lo(e01) * R, bf_hi(e01) * R), y23 = pk_bf16(bf_lo(e23) * R, bf_hi(e23) * R);
  u32x2 o;
  o[0] = pk_bf16(bf_lo(y01) + 1e-6f, bf_hi(y01) + 1e-6f);
  o[1] = pk_bf16(bf_lo(y23) + 1e-6f, bf_hi(y23) + 1e-6f);
  return o;
}

#define STAMP(k)
#define STAMPV(k)
__global__ void __launch_bounds__(NWAVES * 64, 1)
rfa_main(const char* __restrict__ qg, const char* __restrict__ kg, const char* __restrict__ vg, const char* __restrict__ og,
         float* __restrict__ ws) {
  __shared__ __attribute__((aligned(16))) char smem[LDS_BYTES];
  const int lane = threadIdx.x & 63, wave = __builtin_amdgcn_readfirstlane(threadIdx.x >> 6);
  const int g = lane >> 4, c = lane & 15;
  STAMPV(0);
  const int chunk = blockIdx.x;
  const int bh = chunk >> 2, split = chunk & 3;
  const float SCALE = 0.29730177875068026f;
  const u32 lds0 = (u32)(size_t)(LAS char*)smem + wave * WAVE_LDS;
  const u32 aux = lds0 + NSLOT * TILE_B;

  u32 koff[4], voffs[4];
#pragma unroll
  for (int p = 0; p < 4; ++p) {
    const u32 row = 4 * p + g;
    koff[p] = g * 256 + ((c ^ row) << 4);
    voffs[p] = g * 256 + ((c ^ ((row & 7) << 1)) << 4);
  }
  const size_t wg_off = ((size_t)bh * SK + (size_t)split * (SK / NSPLIT)) * (D * 2) + (size_t)wave * TILE_B;
  const char* kbase = kg + wg_off;
  const char* vbase = vg + wg_off;
  constexpr size_t TSTEP = (size_t)NWAVES * TILE_B;

  dma1k(og, lane * 16, aux);
  dma1k(og + 1024, lane * 16, aux + 1024);
  dma1k(qg + bh * (D * 2), c * 16, aux + 2048);
#pragma unroll
  for (int f = 0; f < NSLOT; ++f) fill4k(kbase + f * TSTEP, koff, lds0 + f * TILE_B);

  STAMPV(1);
  u32 kra[4], vra[8];
#pragma unroll
  for (int i = 0; i < 4; ++i) kra[i] = lds0 + c * 256 + (((4 * i + g) ^ c) << 4);
  {
    const u32 row = 4 * g + (c >> 2);
#pragma unroll
    for (int b = 0; b < 8; ++b) vra[b] = lds0 + row * 256 + ((b ^ (row & 7)) << 5) + (c & 3) * 8;
  }

  wait_vm<4 * NSLOT + VTOUCH>();
  u32x4 omX[4], omY[4];
  f32x4 phq;
  {
    u32x4 om[4], qf[4];
#pragma unroll
    for (int i = 0; i < 4; ++i) {
      om[i] = lds_rd128(aux + (c & 7) * 256 + (4 * i + g) * 16);
      qf[i] = lds_rd128(aux + 2048 + (4 * i + g) * 16);
    }
    f32x4 qp = {0.f, 0.f, 0.f, 0.f};
    float qs = 0.f, qs2 = 0.f;
#pragma unroll
    for (int i = 0; i < 4; ++i) {
#pragma unroll
      for (int j = 0; j < 4; ++j) {
        qf[i][j] = scale_pk(qf[i][j], SCALE);
        if (j & 1) qs2 = dot2(qf[i][j], qf[i][j], qs2); else qs = dot2(qf[i][j], qf[i][j], qs);
        omX[i][j] = c < 8 ? om[i][j] : 0u;
        omY[i][j] = c < 8 ? 0u : om[i][j];
      }
      qp = mfma32(om[i], qf[i], qp);
    }
    f32x4 qq = {0.f, 0.f, 0.f, 0.f};
    qq = mfma4f(1.0f, qs + qs2, qq);
    const u32x2 pq = phi4(qp, qq);
    phq[0] = bf_lo(pq[0]); phq[1] = bf_hi(pq[0]); phq[2] = bf_lo(pq[1]); phq[3] = bf_hi(pq[1]);
  }
  const float oneX = c < 8 ? 1.0f : 0.0f, oneY = c < 8 ? 0.0f : 1.0f;
  asm volatile("" : "+v"(phq));
  wait_lgkm0();

  STAMPV(2);
#pragma unroll 1
  for (int p = 0; p < TPW / 2; ++p) {
    const u32 so = (u32)((2 * p) & (NSLOT - 1)) * TILE_B;
    const bool nk = p < (TPW - NSLOT) / 2;
    const char* nsrc = nk ? kbase + (size_t)(2 * p + NSLOT) * TSTEP : vbase + (size_t)(2 * p + NSLOT - TPW) * TSTEP;
    u32 no[4];
#pragma unroll
    for (int i = 0; i < 4; ++i) no[i] = nk ? koff[i] : voffs[i];
    u32x4 kx[4], ky[4];
    wait_vm<4 * (NSLOT - 1)>();
#pragma unroll
    for (int i = 0; i < 4; ++i) kx[i] = lds_rd128(kra[i] + so);
    wait_lgkm0();
    fill4k(nsrc, no, lds0 + so);
    wait_vm<4 * (NSLOT - 1)>();
#pragma unroll
    for (int i = 0; i < 4; ++i) ky[i] = lds_rd128(kra[i] + so + TILE_B);
    wait_lgkm0();
    fill4k(nsrc + TSTEP, no, lds0 + so + TILE_B);
    PIN4(kx); PIN4(ky);

    f32x4 pr = {0.f, 0.f, 0.f, 0.f};
    float sx = 0.f, sy = 0.f, sx2 = 0.f, sy2 = 0.f;
#pragma unroll
    for (int i = 0; i < 4; ++i) {
#pragma unroll
      for (int j = 0; j < 4; ++j) { kx[i][j] = scale_pk(kx[i][j], SCALE); if (j & 1) sx2 = dot2(kx[i][j], kx[i][j], sx2); else sx = dot2(kx[i][j], kx[i][j], sx); }
      pr = mfma32(kx[i], omX[i], pr);
    }
#pragma unroll
    for (int i = 0; i < 4; ++i) {
#pragma unroll
      for (int j = 0; j < 4; ++j) { ky[i][j] = scale_pk(ky[i][j], SCALE); if (j & 1) sy2 = dot2(ky[i][j], ky[i][j], sy2); else sy = dot2(ky[i][j], ky[i][j], sy); }
      pr = mfma32(ky[i], omY[i], pr);
    }
    f32x4 sq = {0.f, 0.f, 0.f, 0.f};
    sq = mfma4f(sx + sx2, oneX, sq);
    sq = mfma4f(sy + sy2, oneY, sq);
    const u32x2 ph = phi4(pr, sq);
    *(LAS u32x2*)(size_t)(aux + p * 512 + lane * 8) = ph;
  }

  STAMPV(3);
  f32x4 acc[8], zacc = {0.f, 0.f, 0.f, 0.f};
#pragma unroll
  for (int b = 0; b < 8; ++b) acc[b] = f32x4{0.f, 0.f, 0.f, 0.f};
  const u32x4 ones = {0x3f803f80u, 0x3f803f80u, 0x3f803f80u, 0x3f803f80u};

  auto vstep = [&](int p, u32 so, auto waitx, auto fillx, auto waity, auto filly) {
    const u32x2 ph = *(const LAS u32x2*)(size_t)(aux + p * 512 + lane * 8);
    u32x4 a;
    a[0] = c < 8 ? ph[0] : 0u; a[1] = c < 8 ? ph[1] : 0u; a[2] = c < 8 ? 0u : ph[0]; a[3] = c < 8 ? 0u : ph[1];
    u32x2 vx[8], vy[8];
    waitx();
#pragma unroll
    for (int b = 0; b < 8; ++b) vx[b] = lds_tr(vra[b] + so);
    fillx();
    waity();
#pragma unroll
    for (int b = 0; b < 8; ++b) vy[b] = lds_tr(vra[b] + so + TILE_B);
    filly();
    PIN8(vx); PIN8(vy);
    zacc = mfma32(a, ones, zacc);
#pragma unroll
    for (int b = 0; b < 8; ++b) {
      const u32x4 bb = {vx[b][0], vx[b][1], vy[b][0], vy[b][1]};
      acc[b] = mfma32(a, bb, acc[b]);
    }
  };
#pragma unroll 1
  for (int p = 0; p < (TPW - NSLOT) / 2; ++p) {
    const u32 so = (u32)((2 * p) & (NSLOT - 1)) * TILE_B;
    const char* nsrc = vbase + (size_t)(2 * p + NSLOT) * TSTEP;
    vstep(p, so, [&] { wait_vm<4 * (NSLOT - 1)>(); }, [&] { wait_lgkm0(); fill4k(nsrc, voffs, lds0 + so); },
          [&] { wait_vm<4 * (NSLOT - 1)>(); }, [&] { wait_lgkm0(); fill4k(nsrc + TSTEP, voffs, lds0 + so + TILE_B); });
  }
  auto nop = [] {};
  vstep(4, 0 * TILE_B, [&] { wait_vm<28>(); }, nop, [&] { wait_vm<24>(); }, nop);
  vstep(5, 2 * TILE_B, [&] { wait_vm<20>(); }, nop, [&] { wait_vm<16>(); }, nop);
  vstep(6, 4 * TILE_B, [&] { wait_vm<12>(); }, nop, [&] { wait_vm<8>(); }, nop);
  vstep(7, 6 * TILE_B, [&] { wait_vm<4>(); }, nop, [&] { wait_vm<0>(); }, nop);

  STAMPV(4);
  float t[8];
#pragma unroll
  for (int b = 0; b < 8; ++b)
    t[b] = fmaf(phq[3], acc[b][3], fmaf(phq[2], acc[b][2], fmaf(phq[1], acc[b][1], phq[0] * acc[b][0])));
  float dn = fmaf(phq[3], zacc[3], fmaf(phq[2], zacc[2], fmaf(phq[1], zacc[1], phq[0] * zacc[0])));
  float u[4];
#pragma unroll
  for (int b = 0; b < 4; ++b) {
    auto s = __builtin_amdgcn_permlane32_swap(__float_as_uint(t[b]), __float_as_uint(t[b + 4]), false, false);
    u[b] = __uint_as_float(s[0]) + __uint_as_float(s[1]);
  }
  float w2[2];
#pragma unroll
  for (int b = 0; b < 2; ++b) {
    auto s = __builtin_amdgcn_permlane16_swap(__float_as_uint(u[b]), __float_as_uint(u[b + 2]), false, false);
    w2[b] = __uint_as_float(s[0]) + __uint_as_float(s[1]);
  }
  {
    auto s = __builtin_amdgcn_permlane32_swap(__float_as_uint(dn), __float_as_uint(dn), false, false);
    dn = __uint_as_float(s[0]) + __uint_as_float(s[1]);
    auto s2 = __builtin_amdgcn_permlane16_swap(__float_as_uint(dn), __float_as_uint(dn), false, false);
    dn = __uint_as_float(s2[0]) + __uint_as_float(s2[1]);
  }
  LAS float* red = (LAS float*)(size_t)lds0;
  red[32 * g + c] = w2[0];
  red[32 * g + 16 + c] = w2[1];
  if (lane == 0) red[128] = dn;
  __syncthreads();
  if (threadIdx.x < 129) {
    const u32 base = (u32)(size_t)(LAS char*)smem;
    float s = 0.f;
#pragma unroll
    for (int w = 0; w < NWAVES; ++w) s += ((const LAS float*)(size_t)(base + w * WAVE_LDS))[threadIdx.x];
    ws[(size_t)chunk * PART_F + threadIdx.x] = s;
  }
}

__global__ void __launch_bounds__(128) rfa_combine(const float* __restrict__ ws, __hip_bfloat16* __restrict__ out) {
  const int bh = blockIdx.x, d = threadIdx.x;
  float num = 0.f, den = 0.f;
#pragma unroll
  for (int s = 0; s < NSPLIT; ++s) {
    const float* p = ws + (size_t)(bh * NSPLIT + s) * PART_F;
    num += p[d]; den += p[128];
  }
  num = __bfloat162float(__float2bfloat16(num));
  den = __bfloat162float(__float2bfloat16(den));
  den = __bfloat162float(__float2bfloat16(den + 1e-6f));
  out[bh * D + d] = __float2bfloat16(num / den);
}

extern "C" void kernel_launch(void* const* d_in, const int* in_sizes, int n_in,
                              void* d_out, int out_size, void* d_ws, size_t ws_size,
                              hipStream_t stream) {
  rfa_main<<<NBH * NSPLIT, NWAVES * 64, 0, stream>>>((const char*)d_in[0], (const char*)d_in[1], (const char*)d_in[2],
                                                      (const char*)d_in[3], (float*)d_ws);
  rfa_combine<<<NBH, 128, 0, stream>>>((const float*)d_ws, (__hip_bfloat16*)d_out);
}
```

```cpp
#include <hip/hip_runtime.h>
#include <hip/hip_bf16.h>
#include <stdint.h>

#define XSWZ 1
#define VTOUCH 0
typedef unsigned int u32;
typedef unsigned long long u64;
using u32x2 = __attribute__((ext_vector_type(2))) unsigned;
using u32x4 = __attribute__((ext_vector_type(4))) unsigned;
using f32x2 = __attribute__((ext_vector_type(2))) float;
using f32x4 = __attribute__((ext_vector_type(4))) float;
using bf16x2 = __attribute__((ext_vector_type(2))) __bf16;
using bf16x8 = __attribute__((ext_vector_type(8))) __bf16;
using s16x4 = __attribute__((ext_vector_type(4))) short;
#define LAS __attribute__((address_space(3)))
#define GAS __attribute__((address_space(1)))

constexpr int NBH = 64, SK = 4096, D = 128, M = 8;
constexpr int NSPLIT = 4, NWAVES = 4;
constexpr int TILE_B = 4096;
constexpr int NSLOT = 8;
constexpr int TPW = SK / NSPLIT / 16 / NWAVES;
constexpr int WAVE_LDS = NSLOT * TILE_B + 4096;
constexpr int CTL_LDS = NWAVES * WAVE_LDS;
constexpr int LDS_BYTES = CTL_LDS + 4096;
constexpr int REC_B = 2048, HEAD_B = 4 * REC_B;
constexpr int OFF_DEN = 1024, OFF_HELLO = 1280, OFF_CHAL = 1536;

__device__ __forceinline__ float bf_lo(u32 x) { return __uint_as_float(x << 16); }
__device__ __forceinline__ float bf_hi(u32 x) { return __uint_as_float(x & 0xffff0000u); }
__device__ __forceinline__ u32 pk_bf16(float a, float b) {
  f32x2 f = {a, b};
  return __builtin_bit_cast(u32, __builtin_convertvector(f, bf16x2));
}
__device__ __forceinline__ u32 scale_pk(u32 x, float s) {
  f32x2 f = {bf_lo(x), bf_hi(x)};
  f = f * s;
  return __builtin_bit_cast(u32, __builtin_convertvector(f, bf16x2));
}
__device__ __forceinline__ float dot2(u32 a, u32 b, float c) {
  return __builtin_amdgcn_fdot2_f32_bf16(__builtin_bit_cast(bf16x2, a), __builtin_bit_cast(bf16x2, b), c, false);
}
__device__ __forceinline__ f32x4 mfma32(u32x4 a, u32x4 b, f32x4 c) {
  return __builtin_amdgcn_mfma_f32_16x16x32_bf16(__builtin_bit_cast(bf16x8, a), __builtin_bit_cast(bf16x8, b), c, 0, 0, 0);
}
__device__ __forceinline__ f32x4 mfma4f(float a, float b, f32x4 c) { return __builtin_amdgcn_mfma_f32_16x16x4f32(a, b, c, 0, 0, 0); }

__device__ __forceinline__ void fill4k(const char* sbase, const u32 (&voff)[4], u32 lds) {
  u32 keep;
  asm volatile("s_mov_b32 %0, m0\n\ts_mov_b32 m0, %6\n\ts_nop 0\n\t"
               "global_load_lds_dwordx4 %1, %5 offset:0 nt\n\t"
               "global_load_lds_dwordx4 %2, %5 offset:1024 nt\n\t"
               "global_load_lds_dwordx4 %3, %5 offset:2048 nt\n\t"
               "global_load_lds_dwordx4 %4, %5 offset:3072 nt\n\t"
               "s_mov_b32 m0, %0"
               : "=&s"(keep) : "v"(voff[0]), "v"(voff[1]), "v"(voff[2]), "v"(voff[3]), "s"(sbase), "s"(lds) : "memory");
}
__device__ __forceinline__ void dma1k(const char* sbase, u32 voff, u32 lds) {
  u32 keep;
  asm volatile("s_mov_b32 %0, m0\n\ts_mov_b32 m0, %3\n\ts_nop 0\n\t"
               "global_load_lds_dwordx4 %1, %2\n\t"
               "s_mov_b32 m0, %0"
               : "=&s"(keep) : "v"(voff), "s"(sbase), "s"(lds) : "memory");
}
__device__ __forceinline__ void dma1k_sc1(const char* sbase, u32 voff, u32 lds) {
  u32 keep;
  asm volatile("s_mov_b32 %0, m0\n\ts_mov_b32 m0, %3\n\ts_nop 0\n\t"
               "global_load_lds_dwordx4 %1, %2 sc1\n\t"
               "s_mov_b32 m0, %0"
               : "=&s"(keep) : "v"(voff), "s"(sbase), "s"(lds) : "memory");
}
__device__ __forceinline__ void st8(char* p, u32 lo, u32 hi) {
  __hip_atomic_store((GAS u64*)p, ((u64)hi << 32) | lo, __ATOMIC_RELAXED, __HIP_MEMORY_SCOPE_AGENT);
}
__device__ __forceinline__ void st4(char* p, u32 v) { __hip_atomic_store((GAS u32*)p, v, __ATOMIC_RELAXED, __HIP_MEMORY_SCOPE_AGENT); }
__device__ __forceinline__ u64 ld8(const char* p) { return __hip_atomic_load((GAS u64*)p, __ATOMIC_RELAXED, __HIP_MEMORY_SCOPE_AGENT); }
__device__ __forceinline__ u32 ld4(const char* p) { return __hip_atomic_load((GAS u32*)p, __ATOMIC_RELAXED, __HIP_MEMORY_SCOPE_AGENT); }
__device__ __forceinline__ float rbf(float x) { return bf_lo(pk_bf16(x, x)); }
template <int N> __device__ __forceinline__ void wait_vm() { asm volatile("s_waitcnt vmcnt(%0)" :: "i"(N) : "memory"); }
__device__ __forceinline__ void wait_lgkm0() { asm volatile("s_waitcnt lgkmcnt(0)" ::: "memory"); }
#define PIN4(x) asm volatile("" : "+v"(x[0]), "+v"(x[1]), "+v"(x[2]), "+v"(x[3]) :: "memory")
#define PIN8(x) asm volatile("" : "+v"(x[0]), "+v"(x[1]), "+v"(x[2]), "+v"(x[3]), "+v"(x[4]), "+v"(x[5]), "+v"(x[6]), "+v"(x[7]) :: "memory")
__device__ __forceinline__ u32x4 lds_rd128(u32 addr) { return *(const LAS u32x4*)(size_t)addr; }
__device__ __forceinline__ u32x2 lds_tr(u32 addr) {
  return __builtin_bit_cast(u32x2, __builtin_amdgcn_ds_read_tr16_b64_v4i16((LAS s16x4*)(size_t)addr));
}

__device__ __forceinline__ u32x2 phi4(f32x4 pr, f32x4 sq) {
  const u32 p01 = pk_bf16(pr[0], pr[1]), p23 = pk_bf16(pr[2], pr[3]);
  const u32 s01 = pk_bf16(sq[0], sq[1]), s23 = pk_bf16(sq[2], sq[3]);
  const u32 x01 = pk_bf16(fmaf(-0.5f, bf_lo(s01), bf_lo(p01)), fmaf(-0.5f, bf_hi(s01), bf_hi(p01)));
  const u32 x23 = pk_bf16(fmaf(-0.5f, bf_lo(s23), bf_lo(p23)), fmaf(-0.5f, bf_hi(s23), bf_hi(p23)));
  const float L2E = 1.4426950408889634f;
  const u32 e01 = pk_bf16(__builtin_amdgcn_exp2f(bf_lo(x01) * L2E), __builtin_amdgcn_exp2f(bf_hi(x01) * L2E));
  const u32 e23 = pk_bf16(__builtin_amdgcn_exp2f(bf_lo(x23) * L2E), __builtin_amdgcn_exp2f(bf_hi(x23) * L2E));
  const float R = 0.35355339059327373f;
  const u32 y01 = pk_bf16(bf_lo(e01) * R, bf_hi(e01) * R), y23 = pk_bf16(bf_lo(e23) * R, bf_hi(e23) * R);
  u32x2 o;
  o[0] = pk_bf16(bf_lo(y01) + 1e-6f, bf_hi(y01) + 1e-6f);
  o[1] = pk_bf16(bf_lo(y23) + 1e-6f, bf_hi(y23) + 1e-6f);
  return o;
}

#define STAMP(k)
#define STAMPV(k)
__global__ void __launch_bounds__(NWAVES * 64, 1)
rfa_main(const char* __restrict__ qg, const char* __restrict__ kg, const char* __restrict__ vg, const char* __restrict__ og,
         char* __restrict__ ws, u32* __restrict__ out) {
  __shared__ __attribute__((aligned(16))) char smem[LDS_BYTES];
  const int lane = threadIdx.x & 63, wave = __builtin_amdgcn_readfirstlane(threadIdx.x >> 6);
  const int g = lane >> 4, c = lane & 15;
  STAMPV(0);
  const int chunk = (blockIdx.x & 7) * 32 + (blockIdx.x >> 3);
  const int bh = chunk >> 2, split = chunk & 3;
  const float SCALE = 0.29730177875068026f;
  const u32 lds0 = (u32)(size_t)(LAS char*)smem + wave * WAVE_LDS;
  const u32 aux = lds0 + NSLOT * TILE_B;

  u32 koff[4], voffs[4];
#pragma unroll
  for (int p = 0; p < 4; ++p) {
    const u32 row = 4 * p + g;
    koff[p] = g * 256 + ((c ^ row) << 4);
    voffs[p] = g * 256 + ((c ^ ((row & 7) << 1)) << 4);
  }
  const size_t wg_off = ((size_t)bh * SK + (size_t)split * (SK / NSPLIT)) * (D * 2) + (size_t)wave * TILE_B;
  const char* kbase = kg + wg_off;
  const char* vbase = vg + wg_off;
  constexpr size_t TSTEP = (size_t)NWAVES * TILE_B;

  const u32 ctl = (u32)(size_t)(LAS char*)smem + CTL_LDS;
  char* hrec = ws + (size_t)bh * HEAD_B;
  char* myrec = hrec + (split - 1) * REC_B;
  const u32 j3 = lane < 2 ? lane : 2;
  if (wave == 0) {
    if (split == 0) {
      dma1k_sc1(hrec, lane * 16, ctl);
      dma1k_sc1(hrec + REC_B, lane * 16, ctl + 1024);
      dma1k_sc1(hrec + 2 * REC_B, lane * 16, ctl + 2048);
      dma1k_sc1(hrec + OFF_DEN, j3 * REC_B, ctl + 3072);
    } else {
      dma1k_sc1(myrec + OFF_HELLO, (lane & 1) * (OFF_CHAL - OFF_HELLO), ctl);
    }
  }
  dma1k(og, lane * 16, aux);
  dma1k(og + 1024, lane * 16, aux + 1024);
  dma1k(qg + bh * (D * 2), c * 16, aux + 2048);
#pragma unroll
  for (int f = 0; f < NSLOT; ++f) fill4k(kbase + f * TSTEP, koff, lds0 + f * TILE_B);

  STAMPV(1);
  u32 kra[4], vra[8];
#pragma unroll
  for (int i = 0; i < 4; ++i) kra[i] = lds0 + c * 256 + (((4 * i + g) ^ c) << 4);
  {
    const u32 row = 4 * g + (c >> 2);
#pragma unroll
    for (int b = 0; b < 8; ++b) vra[b] = lds0 + row * 256 + ((b ^ (row & 7)) << 5) + (c & 3) * 8;
  }

  wait_vm<4 * NSLOT + VTOUCH>();
  u32 tagv = 0;
  if (wave == 0) {
    if (split == 0) {
      const u32x4 a0 = lds_rd128(ctl + lane * 16), a1 = lds_rd128(ctl + 1024 + lane * 16), a2 = lds_rd128(ctl + 2048 + lane * 16), a3 = lds_rd128(ctl + 3072 + lane * 16);
      u32 cand = __builtin_amdgcn_readfirstlane(a0[1]) + 1u;
      while (__any(a0[1] == cand || a0[3] == cand || a1[1] == cand || a1[3] == cand || a2[1] == cand || a2[3] == cand || a3[1] == cand)) ++cand;
      tagv = cand;
      wait_lgkm0();
      dma1k_sc1(hrec + OFF_HELLO, j3 * REC_B, ctl);
    } else {
      const u32 h0 = *(const LAS u32*)(size_t)ctl, e0 = *(const LAS u32*)(size_t)(ctl + 16 + 4);
      u32 pv = __builtin_amdgcn_readfirstlane(h0) + 1u;
      if (pv == (u32)__builtin_amdgcn_readfirstlane(e0)) ++pv;
      tagv = pv;
      if (lane == 0) st4(myrec + OFF_HELLO, pv);
    }
  }
  u32 echoed = 0;
  u32x4 omX[4], omY[4];
  f32x4 phq;
  {
    u32x4 om[4], qf[4];
#pragma unroll
    for (int i = 0; i < 4; ++i) {
      om[i] = lds_rd128(aux + (c & 7) * 256 + (4 * i + g) * 16);
      qf[i] = lds_rd128(aux + 2048 + (4 * i + g) * 16);
    }
    f32x4 qp = {0.f, 0.f, 0.f, 0.f};
    float qs = 0.f, qs2 = 0.f;
#pragma unroll
    for (int i = 0; i < 4; ++i) {
#pragma unroll
      for (int j = 0; j < 4; ++j) {
        qf[i][j] = scale_pk(qf[i][j], SCALE);
        if (j & 1) qs2 = dot2(qf[i][j], qf[i][j], qs2); else qs = dot2(qf[i][j], qf[i][j], qs);
        omX[i][j] = c < 8 ? om[i][j] : 0u;
        omY[i][j] = c < 8 ? 0u : om[i][j];
      }
      qp = mfma32(om[i], qf[i], qp);
    }
    f32x4 qq = {0.f, 0.f, 0.f, 0.f};
    qq = mfma4f(1.0f, qs + qs2, qq);
    const u32x2 pq = phi4(qp, qq);
    phq[0] = bf_lo(pq[0]); phq[1] = bf_hi(pq[0]); phq[2] = bf_lo(pq[1]); phq[3] = bf_hi(pq[1]);
  }
  const float oneX = c < 8 ? 1.0f : 0.0f, oneY = c < 8 ? 0.0f : 1.0f;
  asm volatile("" : "+v"(phq));
  wait_lgkm0();

  STAMPV(2);
#pragma unroll 1
  for (int p = 0; p < TPW / 2; ++p) {
    const u32 so = (u32)((2 * p) & (NSLOT - 1)) * TILE_B;
    const bool nk = p < (TPW - NSLOT) / 2;
    const char* nsrc = nk ? kbase + (size_t)(2 * p + NSLOT) * TSTEP : vbase + (size_t)(2 * p + NSLOT - TPW) * TSTEP;
    u32 no[4];
#pragma unroll
    for (int i = 0; i < 4; ++i) no[i] = nk ? koff[i] : voffs[i];
    u32x4 kx[4], ky[4];
    wait_vm<4 * (NSLOT - 1)>();
#pragma unroll
    for (int i = 0; i < 4; ++i) kx[i] = lds_rd128(kra[i] + so);
    wait_lgkm0();
    fill4k(nsrc, no, lds0 + so);
    wait_vm<4 * (NSLOT - 1)>();
#pragma unroll
    for (int i = 0; i < 4; ++i) ky[i] = lds_rd128(kra[i] + so + TILE_B);
    wait_lgkm0();
    fill4k(nsrc + TSTEP, no, lds0 + so + TILE_B);
    PIN4(kx); PIN4(ky);
    if (p == 4 && wave == 0 && split == 0) {
      echoed = *(const LAS u32*)(size_t)(ctl + lane * 16);
      if (lane < 3) st8(hrec + lane * REC_B + OFF_CHAL, tagv, echoed);
    }

    f32x4 pr = {0.f, 0.f, 0.f, 0.f};
    float sx = 0.f, sy = 0.f, sx2 = 0.f, sy2 = 0.f;
#pragma unroll
    for (int i = 0; i < 4; ++i) {
#pragma unroll
      for (int j = 0; j < 4; ++j) { kx[i][j] = scale_pk(kx[i][j], SCALE); if (j & 1) sx2 = dot2(kx[i][j], kx[i][j], sx2); else sx = dot2(kx[i][j], kx[i][j], sx); }
      pr = mfma32(kx[i], omX[i], pr);
    }
#pragma unroll
    for (int i = 0; i < 4; ++i) {
#pragma unroll
      for (int j = 0; j < 4; ++j) { ky[i][j] = scale_pk(ky[i][j], SCALE); if (j & 1) sy2 = dot2(ky[i][j], ky[i][j], sy2); else sy = dot2(ky[i][j], ky[i][j], sy); }
      pr = mfma32(ky[i], omY[i], pr);
    }
    f32x4 sq = {0.f, 0.f, 0.f, 0.f};
    sq = mfma4f(sx + sx2, oneX, sq);
    sq = mfma4f(sy + sy2, oneY, sq);
    const u32x2 ph = phi4(pr, sq);
    *(LAS u32x2*)(size_t)(aux + p * 512 + lane * 8) = ph;
  }

  STAMPV(3);
  f32x4 acc[8], zacc = {0.f, 0.f, 0.f, 0.f};
#pragma unroll
  for (int b = 0; b < 8; ++b) acc[b] = f32x4{0.f, 0.f, 0.f, 0.f};
  const u32x4 ones = {0x3f803f80u, 0x3f803f80u, 0x3f803f80u, 0x3f803f80u};

  auto vstep = [&](int p, u32 so, auto waitx, auto fillx, auto waity, auto filly) {
    const u32x2 ph = *(const LAS u32x2*)(size_t)(aux + p * 512 + lane * 8);
    u32x4 a;
    a[0] = c < 8 ? ph[0] : 0u; a[1] = c < 8 ? ph[1] : 0u; a[2] = c < 8 ? 0u : ph[0]; a[3] = c < 8 ? 0u : ph[1];
    u32x2 vx[8], vy[8];
    waitx();
#pragma unroll
    for (int b = 0; b < 8; ++b) vx[b] = lds_tr(vra[b] + so);
    fillx();
    waity();
#pragma unroll
    for (int b = 0; b < 8; ++b) vy[b] = lds_tr(vra[b] + so + TILE_B);
    filly();
    PIN8(vx); PIN8(vy);
    zacc = mfma32(a, ones, zacc);
#pragma unroll
    for (int b = 0; b < 8; ++b) {
      const u32x4 bb = {vx[b][0], vx[b][1], vy[b][0], vy[b][1]};
      acc[b] = mfma32(a, bb, acc[b]);
    }
  };
#pragma unroll 1
  for (int p = 0; p < (TPW - NSLOT) / 2; ++p) {
    const u32 so = (u32)((2 * p) & (NSLOT - 1)) * TILE_B;
    const char* nsrc = vbase + (size_t)(2 * p + NSLOT) * TSTEP;
    vstep(p, so, [&] { wait_vm<4 * (NSLOT - 1)>(); }, [&] { wait_lgkm0(); fill4k(nsrc, voffs, lds0 + so); },
          [&] { wait_vm<4 * (NSLOT - 1)>(); }, [&] { wait_lgkm0(); fill4k(nsrc + TSTEP, voffs, lds0 + so + TILE_B); });
  }
  if (wave == 0 && split != 0) dma1k_sc1(myrec + OFF_CHAL, 0u, ctl);
  auto nop = [] {};
  vstep(4, 0 * TILE_B, [&] { wait_vm<28>(); }, nop, [&] { wait_vm<24>(); }, nop);
  vstep(5, 2 * TILE_B, [&] { wait_vm<20>(); }, nop, [&] { wait_vm<16>(); }, nop);
  vstep(6, 4 * TILE_B, [&] { wait_vm<12>(); }, nop, [&] { wait_vm<8>(); }, nop);
  vstep(7, 6 * TILE_B, [&] { wait_vm<4>(); }, nop, [&] { wait_vm<0>(); }, nop);

  STAMPV(4);
  float t[8];
#pragma unroll
  for (int b = 0; b < 8; ++b)
    t[b] = fmaf(phq[3], acc[b][3], fmaf(phq[2], acc[b][2], fmaf(phq[1], acc[b][1], phq[0] * acc[b][0])));
  float dn = fmaf(phq[3], zacc[3], fmaf(phq[2], zacc[2], fmaf(phq[1], zacc[1], phq[0] * zacc[0])));
  float u[4];
#pragma unroll
  for (int b = 0; b < 4; ++b) {
    auto s = __builtin_amdgcn_permlane32_swap(__float_as_uint(t[b]), __float_as_uint(t[b + 4]), false, false);
    u[b] = __uint_as_float(s[0]) + __uint_as_float(s[1]);
  }
  float w2[2];
#pragma unroll
  for (int b = 0; b < 2; ++b) {
    auto s = __builtin_amdgcn_permlane16_swap(__float_as_uint(u[b]), __float_as_uint(u[b + 2]), false, false);
    w2[b] = __uint_as_float(s[0]) + __uint_as_float(s[1]);
  }
  {
    auto s = __builtin_amdgcn_permlane32_swap(__float_as_uint(dn), __float_as_uint(dn), false, false);
    dn = __uint_as_float(s[0]) + __uint_as_float(s[1]);
    auto s2 = __builtin_amdgcn_permlane16_swap(__float_as_uint(dn), __float_as_uint(dn), false, false);
    dn = __uint_as_float(s2[0]) + __uint_as_float(s2[1]);
  }
  LAS float* red = (LAS float*)(size_t)lds0;
  red[32 * g + c] = w2[0];
  red[32 * g + 16 + c] = w2[1];
  if (lane == 0) red[128] = dn;
  __syncthreads();
  if (wave == 0) {
    const u32 base = (u32)(size_t)(LAS char*)smem;
    float n0 = 0.f, n1 = 0.f, dsum = 0.f;
#pragma unroll
    for (int w = 0; w < NWAVES; ++w) {
      const f32x2 x = *(const LAS f32x2*)(size_t)(base + w * WAVE_LDS + lane * 8);
      n0 += x[0]; n1 += x[1]; dsum += *(const LAS float*)(size_t)(base + w * WAVE_LDS + 512);
    }
    const u64 t0 = __builtin_amdgcn_s_memrealtime();
    if (split != 0) {
      const u32x2 cw = *(const LAS u32x2*)(size_t)ctl;
      u32 nn = __builtin_amdgcn_readfirstlane(cw[0]), ee = __builtin_amdgcn_readfirstlane(cw[1]);
      while (ee != tagv) {
        const u64 x = ld8(myrec + OFF_CHAL);
        nn = __builtin_amdgcn_readfirstlane((u32)x); ee = __builtin_amdgcn_readfirstlane((u32)(x >> 32));
        if (__builtin_amdgcn_s_memrealtime() - t0 > 20000000ull) break;
        __builtin_amdgcn_s_sleep(2);
      }
      st8(myrec + lane * 16, __float_as_uint(n0), nn);
      st8(myrec + lane * 16 + 8, __float_as_uint(n1), nn);
      if (lane == 0) st8(myrec + OFF_DEN, __float_as_uint(dsum), nn);
    } else {
      u64 x[9];
      for (;;) {
#pragma unroll
        for (int j = 0; j < 3; ++j) {
          x[3 * j] = ld8(hrec + j * REC_B + lane * 16);
          x[3 * j + 1] = ld8(hrec + j * REC_B + lane * 16 + 8);
          x[3 * j + 2] = ld8(hrec + j * REC_B + OFF_DEN);
        }
        const u32 hv = ld4(hrec + j3 * REC_B + OFF_HELLO);
        bool ok = true;
#pragma unroll
        for (int k = 0; k < 9; ++k) ok = ok && ((u32)(x[k] >> 32) == tagv);
        if (__all(ok)) break;
        if (lane < 3 && hv != echoed) { echoed = hv; st8(hrec + lane * REC_B + OFF_CHAL, tagv, hv); }
        if (__builtin_amdgcn_s_memrealtime() - t0 > 20000000ull) break;
      }
#pragma unroll
      for (int j = 0; j < 3; ++j) {
        n0 += __uint_as_float((u32)x[3 * j]); n1 += __uint_as_float((u32)x[3 * j + 1]); dsum += __uint_as_float((u32)x[3 * j + 2]);
      }
      const float den = rbf(rbf(dsum) + 1e-6f);
      out[bh * (D / 2) + lane] = pk_bf16(rbf(n0) / den, rbf(n1) / den);
    }
  }
}

extern "C" void kernel_launch(void* const* d_in, const int* in_sizes, int n_in,
                              void* d_out, int out_size, void* d_ws, size_t ws_size,
                              hipStream_t stream) {
  rfa_main<<<NBH * NSPLIT, NWAVES * 64, 0, stream>>>((const char*)d_in[0], (const char*)d_in[1], (const char*)d_in[2],
                                                      (const char*)d_in[3], (char*)d_ws, (u32*)d_out);
}
```

```cpp
#include <hip/hip_runtime.h>
#include <hip/hip_bf16.h>
#include <stdint.h>

#define XSWZ 1
#define VTOUCH 0
typedef unsigned int u32;
typedef unsigned long long u64;
using u32x2 = __attribute__((ext_vector_type(2))) unsigned;
using u32x4 = __attribute__((ext_vector_type(4))) unsigned;
using f32x2 = __attribute__((ext_vector_type(2))) float;
using f32x4 = __attribute__((ext_vector_type(4))) float;
using bf16x2 = __attribute__((ext_vector_type(2))) __bf16;
using bf16x8 = __attribute__((ext_vector_type(8))) __bf16;
using s16x4 = __attribute__((ext_vector_type(4))) short;
#define LAS __attribute__((address_space(3)))
#define GAS __attribute__((address_space(1)))

constexpr int NBH = 64, SK = 4096, D = 128, M = 8;
constexpr int NSPLIT = 4, NWAVES = 4;
constexpr int TILE_B = 4096;
constexpr int NSLOT = 8;
constexpr int TPW = SK / NSPLIT / 16 / NWAVES;
constexpr int WAVE_LDS = NSLOT * TILE_B + 4096;
constexpr int LDS_BYTES = NWAVES * WAVE_LDS;
constexpr int PART_F = 132;

__device__ __forceinline__ float bf_lo(u32 x) { return __uint_as_float(x << 16); }
__device__ __forceinline__ float bf_hi(u32 x) { return __uint_as_float(x & 0xffff0000u); }
__device__ __forceinline__ u32 pk_bf16(float a, float b) {
  f32x2 f = {a, b};
  return __builtin_bit_cast(u32, __builtin_convertvector(f, bf16x2));
}
__device__ __forceinline__ u32 scale_pk(u32 x, float s) {
  f32x2 f = {bf_lo(x), bf_hi(x)};
  f = f * s;
  return __builtin_bit_cast(u32, __builtin_convertvector(f, bf16x2));
}
__device__ __forceinline__ float dot2(u32 a, u32 b, float c) {
  return __builtin_amdgcn_fdot2_f32_bf16(__builtin_bit_cast(bf16x2, a), __builtin_bit_cast(bf16x2, b), c, false);
}
__device__ __forceinline__ f32x4 mfma32(u32x4 a, u32x4 b, f32x4 c) {
  return __builtin_amdgcn_mfma_f32_16x16x32_bf16(__builtin_bit_cast(bf16x8, a), __builtin_bit_cast(bf16x8, b), c, 0, 0, 0);
}
__device__ __forceinline__ f32x4 mfma4f(float a, float b, f32x4 c) { return __builtin_amdgcn_mfma_f32_16x16x4f32(a, b, c, 0, 0, 0); }

__device__ __forceinline__ void fill4k(const char* sbase, const u32 (&voff)[4], u32 lds) {
  u32 keep;
  asm volatile("s_mov_b32 %0, m0\n\ts_mov_b32 m0, %6\n\ts_nop 0\n\t"
               "global_load_lds_dwordx4 %1, %5 offset:0 nt\n\t"
               "global_load_lds_dwordx4 %2, %5 offset:1024 nt\n\t"
               "global_load_lds_dwordx4 %3, %5 offset:2048 nt\n\t"
               "global_load_lds_dwordx4 %4, %5 offset:3072 nt\n\t"
               "s_mov_b32 m0, %0"
               : "=&s"(keep) : "v"(voff[0]), "v"(voff[1]), "v"(voff[2]), "v"(voff[3]), "s"(sbase), "s"(lds) : "memory");
}
__device__ __forceinline__ void dma1k(const char* sbase, u32 voff, u32 lds) {
  u32 keep;
  asm volatile("s_mov_b32 %0, m0\n\ts_mov_b32 m0, %3\n\ts_nop 0\n\t"
               "global_load_lds_dwordx4 %1, %2\n\t"
               "s_mov_b32 m0, %0"
               : "=&s"(keep) : "v"(voff), "s"(sbase), "s"(lds) : "memory");
}
template <int N> __device__ __forceinline__ void wait_vm() { asm volatile("s_waitcnt vmcnt(%0)" :: "i"(N) : "memory"); }
__device__ __forceinline__ void wait_lgkm0() { asm volatile("s_waitcnt lgkmcnt(0)" ::: "memory"); }
#define PIN4(x) asm volatile("" : "+v"(x[0]), "+v"(x[1]), "+v"(x[2]), "+v"(x[3]) :: "memory")
#define PIN8(x) asm volatile("" : "+v"(x[0]), "+v"(x[1]), "+v"(x[2]), "+v"(x[3]), "+v"(x[4]), "+v"(x[5]), "+v"(x[6]), "+v"(x[7]) :: "memory")
__device__ __forceinline__ u32x4 lds_rd128(u32 addr) { return *(const LAS u32x4*)(size_t)addr; }
__device__ __forceinline__ u32x2 lds_tr(u32 addr) {
  return __builtin_bit_cast(u32x2, __builtin_amdgcn_ds_read_tr16_b64_v4i16((LAS s16x4*)(size_t)addr));
}

__device__ __forceinline__ u32x2 phi4(f32x4 pr, f32x4 sq) {
  const u32 p01 = pk_bf16(pr[0], pr[1]), p23 = pk_bf16(pr[2], pr[3]);
  const u32 s01 = pk_bf16(sq[0], sq[1]), s23 = pk_bf16(sq[2], sq[3]);
  const u32 x01 = pk_bf16(fmaf(-0.5f, bf_lo(s01), bf_lo(p01)), fmaf(-0.5f, bf_hi(s01), bf_hi(p01)));
  const u32 x23 = pk_bf16(fmaf(-0.5f, bf_lo(s23), bf_lo(p23)), fmaf(-0.5f, bf_hi(s23), bf_hi(p23)));
  const float L2E = 1.4426950408889634f;
  const u32 e01 = pk_bf16(__builtin_amdgcn_exp2f(bf_lo(x01) * L2E), __builtin_amdgcn_exp2f(bf_hi(x01) * L2E));
  const u32 e23 = pk_bf16(__builtin_amdgcn_exp2f(bf_lo(x23) * L2E), __builtin_amdgcn_exp2f(bf_hi(x23) * L2E));
  const float R = 0.35355339059327373f;
  const u32 y01 = pk_bf16(bf_lo(e01) * R, bf_hi(e01) * R), y23 = pk_bf16(bf_lo(e23) * R, bf_hi(e23) * R);
  u32x2 o;
  o[0] = pk_bf16(bf_lo(y01) + 1e-6f, bf_hi(y01) + 1e-6f);
  o[1] = pk_bf16(bf_lo(y23) + 1e-6f, bf_hi(y23) + 1e-6f);
  return o;
}

#define STAMP(k)
#define STAMPV(k)
__global__ void __launch_bounds__(NWAVES * 64, 1)
rfa_main(const char* __restrict__ qg, const char* __restrict__ kg, const char* __restrict__ vg, const char* __restrict__ og,
         float* __restrict__ ws) {
  __shared__ __attribute__((aligned(16))) char smem[LDS_BYTES];
  const int lane = threadIdx.x & 63, wave = __builtin_amdgcn_readfirstlane(threadIdx.x >> 6);
  const int g = lane >> 4, c = lane & 15;
  STAMPV(0);
  const int chunk = (blockIdx.x & 7) * 32 + (blockIdx.x >> 3);
  const int bh = chunk >> 2, split = chunk & 3;
  const float SCALE = 0.29730177875068026f;
  const u32 lds0 = (u32)(size_t)(LAS char*)smem + wave * WAVE_LDS;
  const u32 aux = lds0 + NSLOT * TILE_B;

  u32 koff[4], voffs[4];
#pragma unroll
  for (int p = 0; p < 4; ++p) {
    const u32 row = 4 * p + g;
    koff[p] = g * 256 + ((c ^ row) << 4);
    voffs[p] = g * 256 + ((c ^ ((row & 7) << 1)) << 4);
  }
  const size_t wg_off = ((size_t)bh * SK + (size_t)split * (SK / NSPLIT)) * (D * 2) + (size_t)wave * TILE_B;
  const char* kbase = kg + wg_off;
  const char* vbase = vg + wg_off;
  constexpr size_t TSTEP = (size_t)NWAVES * TILE_B;

  dma1k(og, lane * 16, aux);
  dma1k(og + 1024, lane * 16, aux + 1024);
  dma1k(qg + bh * (D * 2), c * 16, aux + 2048);
#pragma unroll
  for (int f = 0; f < NSLOT; ++f) fill4k(kbase + f * TSTEP, koff, lds0 + f * TILE_B);

  STAMPV(1);
  u32 kra[4], vra[8];
#pragma unroll
  for (int i = 0; i < 4; ++i) kra[i] = lds0 + c * 256 + (((4 * i + g) ^ c) << 4);
  {
    const u32 row = 4 * g + (c >> 2);
#pragma unroll
    for (int b = 0; b < 8; ++b) vra[b] = lds0 + row * 256 + ((b ^ (row & 7)) << 5) + (c & 3) * 8;
  }

  wait_vm<4 * NSLOT + VTOUCH>();
  u32x4 omX[4], omY[4];
  f32x4 phq;
  {
    u32x4 om[4], qf[4];
#pragma unroll
    for (int i = 0; i < 4; ++i) {
      om[i] = lds_rd128(aux + (c & 7) * 256 + (4 * i + g) * 16);
      qf[i] = lds_rd128(aux + 2048 + (4 * i + g) * 16);
    }
    f32x4 qp = {0.f, 0.f, 0.f, 0.f};
    float qs = 0.f, qs2 = 0.f;
#pragma unroll
    for (int i = 0; i < 4; ++i) {
#pragma unroll
      for (int j = 0; j < 4; ++j) {
        qf[i][j] = scale_pk(qf[i][j], SCALE);
        if (j & 1) qs2 = dot2(qf[i][j], qf[i][j], qs2); else qs = dot2(qf[i][j], qf[i][j], qs);
        omX[i][j] = c < 8 ? om[i][j] : 0u;
        omY[i][j] = c < 8 ? 0u : om[i][j];
      }
      qp = mfma32(om[i], qf[i], qp);
    }
    f32x4 qq = {0.f, 0.f, 0.f, 0.f};
    qq = mfma4f(1.0f, qs + qs2, qq);
    const u32x2 pq = phi4(qp, qq);
    phq[0] = bf_lo(pq[0]); phq[1] = bf_hi(pq[0]); phq[2] = bf_lo(pq[1]); phq[3] = bf_hi(pq[1]);
  }
  const float oneX = c < 8 ? 1.0f : 0.0f, oneY = c < 8 ? 0.0f : 1.0f;
  asm volatile("" : "+v"(phq));
  wait_lgkm0();

  STAMPV(2);
#pragma unroll 1
  for (int p = 0; p < TPW / 2; ++p) {
    const u32 so = (u32)((2 * p) & (NSLOT - 1)) * TILE_B;
    const bool nk = p < (TPW - NSLOT) / 2;
    const char* nsrc = nk ? kbase + (size_t)(2 * p + NSLOT) * TSTEP : vbase + (size_t)(2 * p + NSLOT - TPW) * TSTEP;
    u32 no[4];
#pragma unroll
    for (int i = 0; i < 4; ++i) no[i] = nk ? koff[i] : voffs[i];
    u32x4 kx[4], ky[4];
    wait_vm<4 * (NSLOT - 1)>();
#pragma unroll
    for (int i = 0; i < 4; ++i) kx[i] = lds_rd128(kra[i] + so);
    wait_lgkm0();
    fill4k(nsrc, no, lds0 + so);
    wait_vm<4 * (NSLOT - 1)>();
#pragma unroll
    for (int i = 0; i < 4; ++i) ky[i] = lds_rd128(kra[i] + so + TILE_B);
    wait_lgkm0();
    fill4k(nsrc + TSTEP, no, lds0 + so + TILE_B);
    PIN4(kx); PIN4(ky);

    f32x4 pr = {0.f, 0.f, 0.f, 0.f};
    float sx = 0.f, sy = 0.f, sx2 = 0.f, sy2 = 0.f;
#pragma unroll
    for (int i = 0; i < 4; ++i) {
#pragma unroll
      for (int j = 0; j < 4; ++j) { kx[i][j] = scale_pk(kx[i][j], SCALE); if (j & 1) sx2 = dot2(kx[i][j], kx[i][j], sx2); else sx = dot2(kx[i][j], kx[i][j], sx); }
      pr = mfma32(kx[i], omX[i], pr);
    }
#pragma unroll
    for (int i = 0; i < 4; ++i) {
#pragma unroll
      for (int j = 0; j < 4; ++j) { ky[i][j] = scale_pk(ky[i][j], SCALE); if (j & 1) sy2 = dot2(ky[i][j], ky[i][j], sy2); else sy = dot2(ky[i][j], ky[i][j], sy); }
      pr = mfma32(ky[i], omY[i], pr);
    }
    f32x4 sq = {0.f, 0.f, 0.f, 0.f};
    sq = mfma4f(sx + sx2, oneX, sq);
    sq = mfma4f(sy + sy2, oneY, sq);
    const u32x2 ph = phi4(pr, sq);
    *(LAS u32x2*)(size_t)(aux + p * 512 + lane * 8) = ph;
  }

  STAMPV(3);
  f32x4 acc[8], zacc = {0.f, 0.f, 0.f, 0.f};
#pragma unroll
  for (int b = 0; b < 8; ++b) acc[b] = f32x4{0.f, 0.f, 0.f, 0.f};
  const u32x4 ones = {0x3f803f80u, 0x3f803f80u, 0x3f803f80u, 0x3f803f80u};

  auto vstep = [&](int p, u32 so, auto waitx, auto fillx, auto waity, auto filly) {
    const u32x2 ph = *(const LAS u32x2*)(size_t)(aux + p * 512 + lane * 8);
    u32x4 a;
    a[0] = c < 8 ? ph[0] : 0u; a[1] = c < 8 ? ph[1] : 0u; a[2] = c < 8 ? 0u : ph[0]; a[3] = c < 8 ? 0u : ph[1];
    u32x2 vx[8], vy[8];
    waitx();
#pragma unroll
    for (int b = 0; b < 8; ++b) vx[b] = lds_tr(vra[b] + so);
    fillx();
    waity();
#pragma unroll
    for (int b = 0; b < 8; ++b) vy[b] = lds_tr(vra[b] + so + TILE_B);
    filly();
    PIN8(vx); PIN8(vy);
    zacc = mfma32(a, ones, zacc);
#pragma unroll
    for (int b = 0; b < 8; ++b) {
      const u32x4 bb = {vx[b][0], vx[b][1], vy[b][0], vy[b][1]};
      acc[b] = mfma32(a, bb, acc[b]);
    }
  };
#pragma unroll 1
  for (int p = 0; p < (TPW - NSLOT) / 2; ++p) {
    const u32 so = (u32)((2 * p) & (NSLOT - 1)) * TILE_B;
    const char* nsrc = vbase + (size_t)(2 * p + NSLOT) * TSTEP;
    vstep(p, so, [&] { wait_vm<4 * (NSLOT - 1)>(); }, [&] { wait_lgkm0(); fill4k(nsrc, voffs, lds0 + so); },
          [&] { wait_vm<4 * (NSLOT - 1)>(); }, [&] { wait_lgkm0(); fill4k(nsrc + TSTEP, voffs, lds0 + so + TILE_B); });
  }
  auto nop = [] {};
  vstep(4, 0 * TILE_B, [&] { wait_vm<28>(); }, nop, [&] { wait_vm<24>(); }, nop);
  vstep(5, 2 * TILE_B, [&] { wait_vm<20>(); }, nop, [&] { wait_vm<16>(); }, nop);
  vstep(6, 4 * TILE_B, [&] { wait_vm<12>(); }, nop, [&] { wait_vm<8>(); }, nop);
  vstep(7, 6 * TILE_B, [&] { wait_vm<4>(); }, nop, [&] { wait_vm<0>(); }, nop);

  STAMPV(4);
  float t[8];
#pragma unroll
  for (int b = 0; b < 8; ++b)
    t[b] = fmaf(phq[3], acc[b][3], fmaf(phq[2], acc[b][2], fmaf(phq[1], acc[b][1], phq[0] * acc[b][0])));
  float dn = fmaf(phq[3], zacc[3], fmaf(phq[2], zacc[2], fmaf(phq[1], zacc[1], phq[0] * zacc[0])));
  float u[4];
#pragma unroll
  for (int b = 0; b < 4; ++b) {
    auto s = __builtin_amdgcn_permlane32_swap(__float_as_uint(t[b]), __float_as_uint(t[b + 4]), false, false);
    u[b] = __uint_as_float(s[0]) + __uint_as_float(s[1]);
  }
  float w2[2];
#pragma unroll
  for (int b = 0; b < 2; ++b) {
    auto s = __builtin_amdgcn_permlane16_swap(__float_as_uint(u[b]), __float_as_uint(u[b + 2]), false, false);
    w2[b] = __uint_as_float(s[0]) + __uint_as_float(s[1]);
  }
  {
    auto s = __builtin_amdgcn_permlane32_swap(__float_as_uint(dn), __float_as_uint(dn), false, false);
    dn = __uint_as_float(s[0]) + __uint_as_float(s[1]);
    auto s2 = __builtin_amdgcn_permlane16_swap(__float_as_uint(dn), __float_as_uint(dn), false, false);
    dn = __uint_as_float(s2[0]) + __uint_as_float(s2[1]);
  }
  LAS float* red = (LAS float*)(size_t)lds0;
  red[32 * g + c] = w2[0];
  red[32 * g + 16 + c] = w2[1];
  if (lane == 0) red[128] = dn;
  __syncthreads();
  if (threadIdx.x < 129) {
    const u32 base = (u32)(size_t)(LAS char*)smem;
    float s = 0.f;
#pragma unroll
    for (int w = 0; w < NWAVES; ++w) s += ((const LAS float*)(size_t)(base + w * WAVE_LDS))[threadIdx.x];
    ws[(size_t)chunk * PART_F + threadIdx.x] = s;
  }
}

__global__ void __launch_bounds__(128) rfa_combine(const float* __restrict__ ws, __hip_bfloat16* __restrict__ out) {
  const int bh = blockIdx.x, d = threadIdx.x;
  float num = 0.f, den = 0.f;
#pragma unroll
  for (int s = 0; s < NSPLIT; ++s) {
    const float* p = ws + (size_t)(bh * NSPLIT + s) * PART_F;
    num += p[d]; den += p[128];
  }
  num = __bfloat162float(__float2bfloat16(num));
  den = __bfloat162float(__float2bfloat16(den));
  den = __bfloat162float(__float2bfloat16(den + 1e-6f));
  out[bh * D + d] = __float2bfloat16(num / den);
}

extern "C" void kernel_launch(void* const* d_in, const int* in_sizes, int n_in,
                              void* d_out, int out_size, void* d_ws, size_t ws_size,
                              hipStream_t stream) {
  rfa_main<<<NBH * NSPLIT, NWAVES * 64, 0, stream>>>((const char*)d_in[0], (const char*)d_in[1], (const char*)d_in[2],
                                                      (const char*)d_in[3], (float*)d_ws);
  rfa_combine<<<NBH, 128, 0, stream>>>((const float*)d_ws, (__hip_bfloat16*)d_out);
}
```

```cpp
#include <hip/hip_runtime.h>
#include <hip/hip_bf16.h>
#include <stdint.h>

#define XSWZ 1
#define VTOUCH 0
typedef unsigned int u32;
typedef unsigned long long u64;
using u32x2 = __attribute__((ext_vector_type(2))) unsigned;
using u32x4 = __attribute__((ext_vector_type(4))) unsigned;
using f32x2 = __attribute__((ext_vector_type(2))) float;
using f32x4 = __attribute__((ext_vector_type(4))) float;
using bf16x2 = __attribute__((ext_vector_type(2))) __bf16;
using bf16x8 = __attribute__((ext_vector_type(8))) __bf16;
using s16x4 = __attribute__((ext_vector_type(4))) short;
#define LAS __attribute__((address_space(3)))
#define GAS __attribute__((address_space(1)))

constexpr int NBH = 64, SK = 4096, D = 128, M = 8;
constexpr int NSPLIT = 4, NWAVES = 4;
constexpr int TILE_B = 4096;
constexpr int NSLOT = 8;
constexpr int TPW = SK / NSPLIT / 16 / NWAVES;
constexpr int WAVE_LDS = NSLOT * TILE_B + 4096;
constexpr int LDS_BYTES = NWAVES * WAVE_LDS;
constexpr int PART_F = 132;

__device__ __forceinline__ float bf_lo(u32 x) { return __uint_as_float(x << 16); }
__device__ __forceinline__ float bf_hi(u32 x) { return __uint_as_float(x & 0xffff0000u); }
__device__ __forceinline__ u32 pk_bf16(float a, float b) {
  f32x2 f = {a, b};
  return __builtin_bit_cast(u32, __builtin_convertvector(f, bf16x2));
}
__device__ __forceinline__ u32 scale_pk(u32 x, float s) {
  f32x2 f = {bf_lo(x), bf_hi(x)};
  f = f * s;
  return __builtin_bit_cast(u32, __builtin_convertvector(f, bf16x2));
}
__device__ __forceinline__ float dot2(u32 a, u32 b, float c) {
  return __builtin_amdgcn_fdot2_f32_bf16(__builtin_bit_cast(bf16x2, a), __builtin_bit_cast(bf16x2, b), c, false);
}
__device__ __forceinline__ f32x4 mfma32(u32x4 a, u32x4 b, f32x4 c) {
  return __builtin_amdgcn_mfma_f32_16x16x32_bf16(__builtin_bit_cast(bf16x8, a), __builtin_bit_cast(bf16x8, b), c, 0, 0, 0);
}
__device__ __forceinline__ f32x4 mfma4f(float a, float b, f32x4 c) { return __builtin_amdgcn_mfma_f32_16x16x4f32(a, b, c, 0, 0, 0); }

__device__ __forceinline__ void fill4k(const char* sbase, const u32 (&voff)[4], u32 lds) {
  u32 keep;
  asm volatile("s_mov_b32 %0, m0\n\ts_mov_b32 m0, %6\n\ts_nop 0\n\t"
               "global_load_lds_dwordx4 %1, %5 offset:0 nt\n\t"
               "global_load_lds_dwordx4 %2, %5 offset:1024 nt\n\t"
               "global_load_lds_dwordx4 %3, %5 offset:2048 nt\n\t"
               "global_load_lds_dwordx4 %4, %5 offset:3072 nt\n\t"
               "s_mov_b32 m0, %0"
               : "=&s"(keep) : "v"(voff[0]), "v"(voff[1]), "v"(voff[2]), "v"(voff[3]), "s"(sbase), "s"(lds) : "memory");
}
__device__ __forceinline__ void dma1k(const char* sbase, u32 voff, u32 lds) {
  u32 keep;
  asm volatile("s_mov_b32 %0, m0\n\ts_mov_b32 m0, %3\n\ts_nop 0\n\t"
               "global_load_lds_dwordx4 %1, %2\n\t"
               "s_mov_b32 m0, %0"
               : "=&s"(keep) : "v"(voff), "s"(sbase), "s"(lds) : "memory");
}
template <int N> __device__ __forceinline__ void wait_vm() { asm volatile("s_waitcnt vmcnt(%0)" :: "i"(N) : "memory"); }
__device__ __forceinline__ void wait_lgkm0() { asm volatile("s_waitcnt lgkmcnt(0)" ::: "memory"); }
#define PIN4(x) asm volatile("" : "+v"(x[0]), "+v"(x[1]), "+v"(x[2]), "+v"(x[3]) :: "memory")
#define PIN8(x) asm volatile("" : "+v"(x[0]), "+v"(x[1]), "+v"(x[2]), "+v"(x[3]), "+v"(x[4]), "+v"(x[5]), "+v"(x[6]), "+v"(x[7]) :: "memory")
__device__ __forceinline__ u32x4 lds_rd128(u32 addr) { return *(const LAS u32x4*)(size_t)addr; }
__device__ __forceinline__ u32x2 lds_tr(u32 addr) {
  return __builtin_bit_cast(u32x2, __builtin_amdgcn_ds_read_tr16_b64_v4i16((LAS s16x4*)(size_t)addr));
}

__device__ __forceinline__ u32x2 phi4(f32x4 pr, f32x4 sq) {
  const u32 p01 = pk_bf16(pr[0], pr[1]), p23 = pk_bf16(pr[2], pr[3]);
  const u32 s01 = pk_bf16(sq[0], sq[1]), s23 = pk_bf16(sq[2], sq[3]);
  const u32 x01 = pk_bf16(fmaf(-0.5f, bf_lo(s01), bf_lo(p01)), fmaf(-0.5f, bf_hi(s01), bf_hi(p01)));
  const u32 x23 = pk_bf16(fmaf(-0.5f, bf_lo(s23), bf_lo(p23)), fmaf(-0.5f, bf_hi(s23), bf_hi(p23)));
  const float L2E = 1.4426950408889634f;
  const u32 e01 = pk_bf16(__builtin_amdgcn_exp2f(bf_lo(x01) * L2E), __builtin_amdgcn_exp2f(bf_hi(x01) * L2E));
  const u32 e23 = pk_bf16(__builtin_amdgcn_exp2f(bf_lo(x23) * L2E), __builtin_amdgcn_exp2f(bf_hi(x23) * L2E));
  const float R = 0.35355339059327373f;
  const u32 y01 = pk_bf16(bf_lo(e01) * R, bf_hi(e01) * R), y23 = pk_bf16(bf_lo(e23) * R, bf_hi(e23) * R);
  u32x2 o;
  o[0] = pk_bf16(bf_lo(y01) + 1e-6f, bf_hi(y01) + 1e-6f);
  o[1] = pk_bf16(bf_lo(y23) + 1e-6f, bf_hi(y23) + 1e-6f);
  return o;
}

#define STAMP(k)
#define STAMPV(k)
__global__ void __launch_bounds__(NWAVES * 64, 1)
rfa_main(const char* __restrict__ qg, const char* __restrict__ kg, const char* __restrict__ vg, const char* __restrict__ og,
         float* __restrict__ ws) {
  __shared__ __attribute__((aligned(16))) char smem[LDS_BYTES];
  const int lane = threadIdx.x & 63, wave = __builtin_amdgcn_readfirstlane(threadIdx.x >> 6);
  const int g = lane >> 4, c = lane & 15;
  STAMPV(0);
  const int chunk = (blockIdx.x & 7) * 32 + (blockIdx.x >> 3);
  const int bh = chunk >> 2, split = chunk & 3;
  const float SCALE = 0.29730177875068026f;
  const u32 lds0 = (u32)(size_t)(LAS char*)smem + wave * WAVE_LDS;
  const u32 aux = lds0 + NSLOT * TILE_B;

  u32 koff[4], voffs[4];
#pragma unroll
  for (int p = 0; p < 4; ++p) {
    const u32 row = 4 * p + g;
    koff[p] = g * 256 + ((c ^ row) << 4);
    voffs[p] = g * 256 + ((c ^ ((row & 7) << 1)) << 4);
  }
  const size_t wg_off = ((size_t)bh * SK + (size_t)split * (SK / NSPLIT)) * (D * 2) + (size_t)wave * TILE_B;
  const char* kbase = kg + wg_off;
  const char* vbase = vg + wg_off;
  constexpr size_t TSTEP = (size_t)NWAVES * TILE_B;

  dma1k(og, lane * 16, aux);
  dma1k(og + 1024, lane * 16, aux + 1024);
  dma1k(qg + bh * (D * 2), c * 16, aux + 2048);
#pragma unroll
  for (int f = 0; f < NSLOT; ++f) fill4k(kbase + f * TSTEP, koff, lds0 + f * TILE_B);

  STAMPV(1);
  u32 kra[4], vra[8];
#pragma unroll
  for (int i = 0; i < 4; ++i) kra[i] = lds0 + c * 256 + (((4 * i + g) ^ c) << 4);
  {
    const u32 row = 4 * g + (c >> 2);
#pragma unroll
    for (int b = 0; b < 8; ++b) vra[b] = lds0 + row * 256 + ((b ^ (row & 7)) << 5) + (c & 3) * 8;
  }

  wait_vm<4 * NSLOT + VTOUCH>();
  u32x4 omX[4], omY[4];
  f32x4 phq;
  {
    u32x4 om[4], qf[4];
#pragma unroll
    for (int i = 0; i < 4; ++i) {
      om[i] = lds_rd128(aux + (c & 7) * 256 + (4 * i + g) * 16);
      qf[i] = lds_rd128(aux + 2048 + (4 * i + g) * 16);
    }
    f32x4 qp = {0.f, 0.f, 0.f, 0.f};
    float qs = 0.f, qs2 = 0.f;
#pragma unroll
    for (int i = 0; i < 4; ++i) {
#pragma unroll
      for (int j = 0; j < 4; ++j) {
        qf[i][j] = scale_pk(qf[i][j], SCALE);
        if (j & 1) qs2 = dot2(qf[i][j], qf[i][j], qs2); else qs = dot2(qf[i][j], qf[i][j], qs);
        omX[i][j] = c < 8 ? om[i][j] : 0u;
        omY[i][j] = c < 8 ? 0u : om[i][j];
      }
      qp = mfma32(om[i], qf[i], qp);
    }
    f32x4 qq = {0.f, 0.f, 0.f, 0.f};
    qq = mfma4f(1.0f, qs + qs2, qq);
    const u32x2 pq = phi4(qp, qq);
    phq[0] = bf_lo(pq[0]); phq[1] = bf_hi(pq[0]); phq[2] = bf_lo(pq[1]); phq[3] = bf_hi(pq[1]);
  }
  const float oneX = c < 8 ? 1.0f : 0.0f, oneY = c < 8 ? 0.0f : 1.0f;
  asm volatile("" : "+v"(phq));
  wait_lgkm0();

  STAMPV(2);
#pragma unroll 1
  for (int p = 0; p < TPW / 2; ++p) {
    const u32 so = (u32)((2 * p) & (NSLOT - 1)) * TILE_B;
    const bool nk = p < (TPW - NSLOT) / 2;
    const char* nsrc = nk ? kbase + (size_t)(2 * p + NSLOT) * TSTEP : vbase + (size_t)(2 * p + NSLOT - TPW) * TSTEP;
    u32 no[4];
#pragma unroll
    for (int i = 0; i < 4; ++i) no[i] = nk ? koff[i] : voffs[i];
    u32x4 kx[4], ky[4];
    wait_vm<4 * (NSLOT - 1)>();
#pragma unroll
    for (int i = 0; i < 4; ++i) kx[i] = lds_rd128(kra[i] + so);
    wait_lgkm0();
    fill4k(nsrc, no, lds0 + so);
    wait_vm<4 * (NSLOT - 1)>();
#pragma unroll
    for (int i = 0; i < 4; ++i) ky[i] = lds_rd128(kra[i] + so + TILE_B);
    wait_lgkm0();
    fill4k(nsrc + TSTEP, no, lds0 + so + TILE_B);
    PIN4(kx); PIN4(ky);

    f32x4 pr = {0.f, 0.f, 0.f, 0.f};
    float sx = 0.f, sy = 0.f, sx2 = 0.f, sy2 = 0.f;
#pragma unroll
    for (int i = 0; i < 4; ++i) {
#pragma unroll
      for (int j = 0; j < 4; ++j) { kx[i][j] = scale_pk(kx[i][j], SCALE); if (j & 1) sx2 = dot2(kx[i][j], kx[i][j], sx2); else sx = dot2(kx[i][j], kx[i][j], sx); }
      pr = mfma32(kx[i], omX[i], pr);
    }
#pragma unroll
    for (int i = 0; i < 4; ++i) {
#pragma unroll
      for (int j = 0; j < 4; ++j) { ky[i][j] = scale_pk(ky[i][j], SCALE); if (j & 1) sy2 = dot2(ky[i][j], ky[i][j], sy2); else sy = dot2(ky[i][j], ky[i][j], sy); }
      pr = mfma32(ky[i], omY[i], pr);
    }
    {
      u32x4 dum[4];
#pragma unroll
      for (int rep = 0; rep < 2; ++rep) {
#pragma unroll
      for (int i = 0; i < 4; ++i) { dum[i] = rep ? ky[i] : kx[i];
#pragma unroll
        for (int j = 0; j < 4; ++j) dum[i][j] = scale_pk(dum[i][j], 1.25f); }
      PIN4(dum);
      }
    }
    f32x4 sq = {0.f, 0.f, 0.f, 0.f};
    sq = mfma4f(sx + sx2, oneX, sq);
    sq = mfma4f(sy + sy2, oneY, sq);
    const u32x2 ph = phi4(pr, sq);
    *(LAS u32x2*)(size_t)(aux + p * 512 + lane * 8) = ph;
  }

  STAMPV(3);
  f32x4 acc[8], zacc = {0.f, 0.f, 0.f, 0.f};
#pragma unroll
  for (int b = 0; b < 8; ++b) acc[b] = f32x4{0.f, 0.f, 0.f, 0.f};
  const u32x4 ones = {0x3f803f80u, 0x3f803f80u, 0x3f803f80u, 0x3f803f80u};

  auto vstep = [&](int p, u32 so, auto waitx, auto fillx, auto waity, auto filly) {
    const u32x2 ph = *(const LAS u32x2*)(size_t)(aux + p * 512 + lane * 8);
    u32x4 a;
    a[0] = c < 8 ? ph[0] : 0u; a[1] = c < 8 ? ph[1] : 0u; a[2] = c < 8 ? 0u : ph[0]; a[3] = c < 8 ? 0u : ph[1];
    u32x2 vx[8], vy[8];
    waitx();
#pragma unroll
    for (int b = 0; b < 8; ++b) vx[b] = lds_tr(vra[b] + so);
    fillx();
    waity();
#pragma unroll
    for (int b = 0; b < 8; ++b) vy[b] = lds_tr(vra[b] + so + TILE_B);
    filly();
    PIN8(vx); PIN8(vy);
    zacc = mfma32(a, ones, zacc);
#pragma unroll
    for (int b = 0; b < 8; ++b) {
      const u32x4 bb = {vx[b][0], vx[b][1], vy[b][0], vy[b][1]};
      acc[b] = mfma32(a, bb, acc[b]);
    }
  };
#pragma unroll 1
  for (int p = 0; p < (TPW - NSLOT) / 2; ++p) {
    const u32 so = (u32)((2 * p) & (NSLOT - 1)) * TILE_B;
    const char* nsrc = vbase + (size_t)(2 * p + NSLOT) * TSTEP;
    vstep(p, so, [&] { wait_vm<4 * (NSLOT - 1)>(); }, [&] { wait_lgkm0(); fill4k(nsrc, voffs, lds0 + so); },
          [&] { wait_vm<4 * (NSLOT - 1)>(); }, [&] { wait_lgkm0(); fill4k(nsrc + TSTEP, voffs, lds0 + so + TILE_B); });
  }
  auto nop = [] {};
  vstep(4, 0 * TILE_B, [&] { wait_vm<28>(); }, nop, [&] { wait_vm<24>(); }, nop);
  vstep(5, 2 * TILE_B, [&] { wait_vm<20>(); }, nop, [&] { wait_vm<16>(); }, nop);
  vstep(6, 4 * TILE_B, [&] { wait_vm<12>(); }, nop, [&] { wait_vm<8>(); }, nop);
  vstep(7, 6 * TILE_B, [&] { wait_vm<4>(); }, nop, [&] { wait_vm<0>(); }, nop);

  STAMPV(4);
  float t[8];
#pragma unroll
  for (int b = 0; b < 8; ++b)
    t[b] = fmaf(phq[3], acc[b][3], fmaf(phq[2], acc[b][2], fmaf(phq[1], acc[b][1], phq[0] * acc[b][0])));
  float dn = fmaf(phq[3], zacc[3], fmaf(phq[2], zacc[2], fmaf(phq[1], zacc[1], phq[0] * zacc[0])));
  float u[4];
#pragma unroll
  for (int b = 0; b < 4; ++b) {
    auto s = __builtin_amdgcn_permlane32_swap(__float_as_uint(t[b]), __float_as_uint(t[b + 4]), false, false);
    u[b] = __uint_as_float(s[0]) + __uint_as_float(s[1]);
  }
  float w2[2];
#pragma unroll
  for (int b = 0; b < 2; ++b) {
    auto s = __builtin_amdgcn_permlane16_swap(__float_as_uint(u[b]), __float_as_uint(u[b + 2]), false, false);
    w2[b] = __uint_as_float(s[0]) + __uint_as_float(s[1]);
  }
  {
    auto s = __builtin_amdgcn_permlane32_swap(__float_as_uint(dn), __float_as_uint(dn), false, false);
    dn = __uint_as_float(s[0]) + __uint_as_float(s[1]);
    auto s2 = __builtin_amdgcn_permlane16_swap(__float_as_uint(dn), __float_as_uint(dn), false, false);
    dn = __uint_as_float(s2[0]) + __uint_as_float(s2[1]);
  }
  LAS float* red = (LAS float*)(size_t)lds0;
  red[32 * g + c] = w2[0];
  red[32 * g + 16 + c] = w2[1];
  if (lane == 0) red[128] = dn;
  __syncthreads();
  if (threadIdx.x < 129) {
    const u32 base = (u32)(size_t)(LAS char*)smem;
    float s = 0.f;
#pragma unroll
    for (int w = 0; w < NWAVES; ++w) s += ((const LAS float*)(size_t)(base + w * WAVE_LDS))[threadIdx.x];
    ws[(size_t)chunk * PART_F + threadIdx.x] = s;
  }
}

__global__ void __launch_bounds__(128) rfa_combine(const float* __restrict__ ws, __hip_bfloat16* __restrict__ out) {
  const int bh = blockIdx.x, d = threadIdx.x;
  float num = 0.f, den = 0.f;
#pragma unroll
  for (int s = 0; s < NSPLIT; ++s) {
    const float* p = ws + (size_t)(bh * NSPLIT + s) * PART_F;
    num += p[d]; den += p[128];
  }
  num = __bfloat162float(__float2bfloat16(num));
  den = __bfloat162float(__float2bfloat16(den));
  den = __bfloat162float(__float2bfloat16(den + 1e-6f));
  out[bh * D + d] = __float2bfloat16(num / den);
}

extern "C" void kernel_launch(void* const* d_in, const int* in_sizes, int n_in,
                              void* d_out, int out_size, void* d_ws, size_t ws_size,
                              hipStream_t stream) {
  rfa_main<<<NBH * NSPLIT, NWAVES * 64, 0, stream>>>((const char*)d_in[0], (const char*)d_in[1], (const char*)d_in[2],
                                                      (const char*)d_in[3], (float*)d_ws);
  rfa_combine<<<NBH, 128, 0, stream>>>((const float*)d_ws, (__hip_bfloat16*)d_out);
}
```

```cpp
#include <hip/hip_runtime.h>
#include <hip/hip_bf16.h>
#include <stdint.h>
#include <stdio.h>

#define XSWZ 1
#define VTOUCH 0
typedef unsigned int u32;
typedef unsigned long long u64;
using u32x2 = __attribute__((ext_vector_type(2))) unsigned;
using u32x4 = __attribute__((ext_vector_type(4))) unsigned;
using f32x2 = __attribute__((ext_vector_type(2))) float;
using f32x4 = __attribute__((ext_vector_type(4))) float;
using bf16x2 = __attribute__((ext_vector_type(2))) __bf16;
using bf16x8 = __attribute__((ext_vector_type(8))) __bf16;
using s16x4 = __attribute__((ext_vector_type(4))) short;
#define LAS __attribute__((address_space(3)))
#define GAS __attribute__((address_space(1)))

constexpr int NBH = 64, SK = 4096, D = 128, M = 8;
constexpr int NSPLIT = 4, NWAVES = 4;
constexpr int TILE_B = 4096;
constexpr int NSLOT = 8;
constexpr int TPW = SK / NSPLIT / 16 / NWAVES;
constexpr int WAVE_LDS = NSLOT * TILE_B + 4096;
constexpr int LDS_BYTES = NWAVES * WAVE_LDS;
constexpr int PART_F = 132;

__device__ __forceinline__ float bf_lo(u32 x) { return __uint_as_float(x << 16); }
__device__ __forceinline__ float bf_hi(u32 x) { return __uint_as_float(x & 0xffff0000u); }
__device__ __forceinline__ u32 pk_bf16(float a, float b) {
  f32x2 f = {a, b};
  return __builtin_bit_cast(u32, __builtin_convertvector(f, bf16x2));
}
__device__ __forceinline__ u32 scale_pk(u32 x, float s) {
  f32x2 f = {bf_lo(x), bf_hi(x)};
  f = f * s;
  return __builtin_bit_cast(u32, __builtin_convertvector(f, bf16x2));
}
__device__ __forceinline__ float dot2(u32 a, u32 b, float c) {
  return __builtin_amdgcn_fdot2_f32_bf16(__builtin_bit_cast(bf16x2, a), __builtin_bit_cast(bf16x2, b), c, false);
}
__device__ __forceinline__ f32x4 mfma32(u32x4 a, u32x4 b, f32x4 c) {
  return __builtin_amdgcn_mfma_f32_16x16x32_bf16(__builtin_bit_cast(bf16x8, a), __builtin_bit_cast(bf16x8, b), c, 0, 0, 0);
}
__device__ __forceinline__ f32x4 mfma4f(float a, float b, f32x4 c) { return __builtin_amdgcn_mfma_f32_16x16x4f32(a, b, c, 0, 0, 0); }

__device__ __forceinline__ void fill4k(const char* sbase, const u32 (&voff)[4], u32 lds) {
  u32 keep;
  asm volatile("s_mov_b32 %0, m0\n\ts_mov_b32 m0, %6\n\ts_nop 0\n\t"
               "global_load_lds_dwordx4 %1, %5 offset:0 nt\n\t"
               "global_load_lds_dwordx4 %2, %5 offset:1024 nt\n\t"
               "global_load_lds_dwordx4 %3, %5 offset:2048 nt\n\t"
               "global_load_lds_dwordx4 %4, %5 offset:3072 nt\n\t"
               "s_mov_b32 m0, %0"
               : "=&s"(keep) : "v"(voff[0]), "v"(voff[1]), "v"(voff[2]), "v"(voff[3]), "s"(sbase), "s"(lds) : "memory");
}
__device__ __forceinline__ void dma1k(const char* sbase, u32 voff, u32 lds) {
  u32 keep;
  asm volatile("s_mov_b32 %0, m0\n\ts_mov_b32 m0, %3\n\ts_nop 0\n\t"
               "global_load_lds_dwordx4 %1, %2\n\t"
               "s_mov_b32 m0, %0"
               : "=&s"(keep) : "v"(voff), "s"(sbase), "s"(lds) : "memory");
}
template <int N> __device__ __forceinline__ void wait_vm() { asm volatile("s_waitcnt vmcnt(%0)" :: "i"(N) : "memory"); }
__device__ __forceinline__ void wait_lgkm0() { asm volatile("s_waitcnt lgkmcnt(0)" ::: "memory"); }
#define PIN4(x) asm volatile("" : "+v"(x[0]), "+v"(x[1]), "+v"(x[2]), "+v"(x[3]) :: "memory")
#define PIN8(x) asm volatile("" : "+v"(x[0]), "+v"(x[1]), "+v"(x[2]), "+v"(x[3]), "+v"(x[4]), "+v"(x[5]), "+v"(x[6]), "+v"(x[7]) :: "memory")
__device__ __forceinline__ u32x4 lds_rd128(u32 addr) { return *(const LAS u32x4*)(size_t)addr; }
__device__ __forceinline__ u32x2 lds_tr(u32 addr) {
  return __builtin_bit_cast(u32x2, __builtin_amdgcn_ds_read_tr16_b64_v4i16((LAS s16x4*)(size_t)addr));
}

__device__ __forceinline__ u32x2 phi4(f32x4 pr, f32x4 sq) {
  const u32 p01 = pk_bf16(pr[0], pr[1]), p23 = pk_bf16(pr[2], pr[3]);
  const u32 s01 = pk_bf16(sq[0], sq[1]), s23 = pk_bf16(sq[2], sq[3]);
  const u32 x01 = pk_bf16(fmaf(-0.5f, bf_lo(s01), bf_lo(p01)), fmaf(-0.5f, bf_hi(s01), bf_hi(p01)));
  const u32 x23 = pk_bf16(fmaf(-0.5f, bf_lo(s23), bf_lo(p23)), fmaf(-0.5f, bf_hi(s23), bf_hi(p23)));
  const float L2E = 1.4426950408889634f;
  const u32 e01 = pk_bf16(__builtin_amdgcn_exp2f(bf_lo(x01) * L2E), __builtin_amdgcn_exp2f(bf_hi(x01) * L2E));
  const u32 e23 = pk_bf16(__builtin_amdgcn_exp2f(bf_lo(x23) * L2E), __builtin_amdgcn_exp2f(bf_hi(x23) * L2E));
  const float R = 0.35355339059327373f;
  const u32 y01 = pk_bf16(bf_lo(e01) * R, bf_hi(e01) * R), y23 = pk_bf16(bf_lo(e23) * R, bf_hi(e23) * R);
  u32x2 o;
  o[0] = pk_bf16(bf_lo(y01) + 1e-6f, bf_hi(y01) + 1e-6f);
  o[1] = pk_bf16(bf_lo(y23) + 1e-6f, bf_hi(y23) + 1e-6f);
  return o;
}

#define STAMP(k)
#define STAMPV(k)
__global__ void __launch_bounds__(NWAVES * 64, 1)
rfa_main(const char* __restrict__ qg, const char* __restrict__ kg, const char* __restrict__ vg, const char* __restrict__ og,
         float* __restrict__ ws) {
  __shared__ __attribute__((aligned(16))) char smem[LDS_BYTES];
  const int lane = threadIdx.x & 63, wave = __builtin_amdgcn_readfirstlane(threadIdx.x >> 6);
  const int g = lane >> 4, c = lane & 15;
  STAMPV(0);
  const int chunk = (blockIdx.x & 7) * 32 + (blockIdx.x >> 3);
  const int bh = chunk >> 2, split = chunk & 3;
  const float SCALE = 0.29730177875068026f;
  const u32 lds0 = (u32)(size_t)(LAS char*)smem + wave * WAVE_LDS;
  const u32 aux = lds0 + NSLOT * TILE_B;

  u32 koff[4], voffs[4];
#pragma unroll
  for (int p = 0; p < 4; ++p) {
    const u32 row = 4 * p + g;
    koff[p] = g * 256 + ((c ^ row) << 4);
    voffs[p] = g * 256 + ((c ^ ((row & 7) << 1)) << 4);
  }
  const size_t wg_off = ((size_t)bh * SK + (size_t)split * (SK / NSPLIT)) * (D * 2) + (size_t)wave * TILE_B;
  const char* kbase = kg + wg_off;
  const char* vbase = vg + wg_off;
  constexpr size_t TSTEP = (size_t)NWAVES * TILE_B;

  dma1k(og, lane * 16, aux);
  dma1k(og + 1024, lane * 16, aux + 1024);
  dma1k(qg + bh * (D * 2), c * 16, aux + 2048);
#pragma unroll
  for (int f = 0; f < NSLOT; ++f) fill4k(kbase + f * TSTEP, koff, lds0 + f * TILE_B);

  STAMPV(1);
  u32 kra[4], vra[8];
#pragma unroll
  for (int i = 0; i < 4; ++i) kra[i] = lds0 + c * 256 + (((4 * i + g) ^ c) << 4);
  {
    const u32 row = 4 * g + (c >> 2);
#pragma unroll
    for (int b = 0; b < 8; ++b) vra[b] = lds0 + row * 256 + ((b ^ (row & 7)) << 5) + (c & 3) * 8;
  }

  wait_vm<4 * NSLOT + VTOUCH>();
  u32x4 omX[4], omY[4];
  f32x4 phq;
  {
    u32x4 om[4], qf[4];
#pragma unroll
    for (int i = 0; i < 4; ++i) {
      om[i] = lds_rd128(aux + (c & 7) * 256 + (4 * i + g) * 16);
      qf[i] = lds_rd128(aux + 2048 + (4 * i + g) * 16);
    }
    f32x4 qp = {0.f, 0.f, 0.f, 0.f};
    float qs = 0.f, qs2 = 0.f;
#pragma unroll
    for (int i = 0; i < 4; ++i) {
#pragma unroll
      for (int j = 0; j < 4; ++j) {
        qf[i][j] = scale_pk(qf[i][j], SCALE);
        if (j & 1) qs2 = dot2(qf[i][j], qf[i][j], qs2); else qs = dot2(qf[i][j], qf[i][j], qs);
        omX[i][j] = c < 8 ? om[i][j] : 0u;
        omY[i][j] = c < 8 ? 0u : om[i][j];
      }
      qp = mfma32(om[i], qf[i], qp);
    }
    f32x4 qq = {0.f, 0.f, 0.f, 0.f};
    qq = mfma4f(1.0f, qs + qs2, qq);
    const u32x2 pq = phi4(qp, qq);
    phq[0] = bf_lo(pq[0]); phq[1] = bf_hi(pq[0]); phq[2] = bf_lo(pq[1]); phq[3] = bf_hi(pq[1]);
  }
  const float oneX = c < 8 ? 1.0f : 0.0f, oneY = c < 8 ? 0.0f : 1.0f;
  asm volatile("" : "+v"(phq));
  wait_lgkm0();

  STAMPV(2);
#pragma unroll 1
  for (int p = 0; p < TPW / 2; ++p) {
    const u32 so = (u32)((2 * p) & (NSLOT - 1)) * TILE_B;
    const bool nk = p < (TPW - NSLOT) / 2;
    const char* nsrc = nk ? kbase + (size_t)(2 * p + NSLOT) * TSTEP : vbase + (size_t)(2 * p + NSLOT - TPW) * TSTEP;
    u32 no[4];
#pragma unroll
    for (int i = 0; i < 4; ++i) no[i] = nk ? koff[i] : voffs[i];
    u32x4 kx[4], ky[4];
    wait_vm<4 * (NSLOT - 1)>();
#pragma unroll
    for (int i = 0; i < 4; ++i) kx[i] = lds_rd128(kra[i] + so);
    wait_lgkm0();
    fill4k(nsrc, no, lds0 + so);
    wait_vm<4 * (NSLOT - 1)>();
#pragma unroll
    for (int i = 0; i < 4; ++i) ky[i] = lds_rd128(kra[i] + so + TILE_B);
    wait_lgkm0();
    fill4k(nsrc + TSTEP, no, lds0 + so + TILE_B);
    PIN4(kx); PIN4(ky);

    f32x4 pr = {0.f, 0.f, 0.f, 0.f};
    float sx = 0.f, sy = 0.f, sx2 = 0.f, sy2 = 0.f;
#pragma unroll
    for (int i = 0; i < 4; ++i) {
#pragma unroll
      for (int j = 0; j < 4; ++j) { kx[i][j] = scale_pk(kx[i][j], SCALE); if (j & 1) sx2 = dot2(kx[i][j], kx[i][j], sx2); else sx = dot2(kx[i][j], kx[i][j], sx); }
      pr = mfma32(kx[i], omX[i], pr);
    }
#pragma unroll
    for (int i = 0; i < 4; ++i) {
#pragma unroll
      for (int j = 0; j < 4; ++j) { ky[i][j] = scale_pk(ky[i][j], SCALE); if (j & 1) sy2 = dot2(ky[i][j], ky[i][j], sy2); else sy = dot2(ky[i][j], ky[i][j], sy); }
      pr = mfma32(ky[i], omY[i], pr);
    }
    f32x4 sq = {0.f, 0.f, 0.f, 0.f};
    sq = mfma4f(sx + sx2, oneX, sq);
    sq = mfma4f(sy + sy2, oneY, sq);
    const u32x2 ph = phi4(pr, sq);
    *(LAS u32x2*)(size_t)(aux + p * 512 + lane * 8) = ph;
  }

  STAMPV(3);
  f32x4 acc[8], zacc = {0.f, 0.f, 0.f, 0.f};
#pragma unroll
  for (int b = 0; b < 8; ++b) acc[b] = f32x4{0.f, 0.f, 0.f, 0.f};
  const u32x4 ones = {0x3f803f80u, 0x3f803f80u, 0x3f803f80u, 0x3f803f80u};

  auto vstep = [&](int p, u32 so, auto waitx, auto fillx, auto waity, auto filly) {
    const u32x2 ph = *(const LAS u32x2*)(size_t)(aux + p * 512 + lane * 8);
    u32x4 a;
    a[0] = c < 8 ? ph[0] : 0u; a[1] = c < 8 ? ph[1] : 0u; a[2] = c < 8 ? 0u : ph[0]; a[3] = c < 8 ? 0u : ph[1];
    u32x2 vx[8], vy[8];
    waitx();
#pragma unroll
    for (int b = 0; b < 8; ++b) vx[b] = lds_tr(vra[b] + so);
    fillx();
    waity();
#pragma unroll
    for (int b = 0; b < 8; ++b) vy[b] = lds_tr(vra[b] + so + TILE_B);
    filly();
    PIN8(vx); PIN8(vy);
    zacc = mfma32(a, ones, zacc);
#pragma unroll
    for (int b = 0; b < 8; ++b) {
      const u32x4 bb = {vx[b][0], vx[b][1], vy[b][0], vy[b][1]};
      acc[b] = mfma32(a, bb, acc[b]);
    }
  };
#pragma unroll 1
  for (int p = 0; p < (TPW - NSLOT) / 2; ++p) {
    const u32 so = (u32)((2 * p) & (NSLOT - 1)) * TILE_B;
    const char* nsrc = vbase + (size_t)(2 * p + NSLOT) * TSTEP;
    vstep(p, so, [&] { wait_vm<4 * (NSLOT - 1)>(); }, [&] { wait_lgkm0(); fill4k(nsrc, voffs, lds0 + so); },
          [&] { wait_vm<4 * (NSLOT - 1)>(); }, [&] { wait_lgkm0(); fill4k(nsrc + TSTEP, voffs, lds0 + so + TILE_B); });
  }
  auto nop = [] {};
  vstep(4, 0 * TILE_B, [&] { wait_vm<28>(); }, nop, [&] { wait_vm<24>(); }, nop);
  vstep(5, 2 * TILE_B, [&] { wait_vm<20>(); }, nop, [&] { wait_vm<16>(); }, nop);
  vstep(6, 4 * TILE_B, [&] { wait_vm<12>(); }, nop, [&] { wait_vm<8>(); }, nop);
  vstep(7, 6 * TILE_B, [&] { wait_vm<4>(); }, nop, [&] { wait_vm<0>(); }, nop);

  STAMPV(4);
  float t[8];
#pragma unroll
  for (int b = 0; b < 8; ++b)
    t[b] = fmaf(phq[3], acc[b][3], fmaf(phq[2], acc[b][2], fmaf(phq[1], acc[b][1], phq[0] * acc[b][0])));
  float dn = fmaf(phq[3], zacc[3], fmaf(phq[2], zacc[2], fmaf(phq[1], zacc[1], phq[0] * zacc[0])));
  float u[4];
#pragma unroll
  for (int b = 0; b < 4; ++b) {
    auto s = __builtin_amdgcn_permlane32_swap(__float_as_uint(t[b]), __float_as_uint(t[b + 4]), false, false);
    u[b] = __uint_as_float(s[0]) + __uint_as_float(s[1]);
  }
  float w2[2];
#pragma unroll
  for (int b = 0; b < 2; ++b) {
    auto s = __builtin_amdgcn_permlane16_swap(__float_as_uint(u[b]), __float_as_uint(u[b + 2]), false, false);
    w2[b] = __uint_as_float(s[0]) + __uint_as_float(s[1]);
  }
  {
    auto s = __builtin_amdgcn_permlane32_swap(__float_as_uint(dn), __float_as_uint(dn), false, false);
    dn = __uint_as_float(s[0]) + __uint_as_float(s[1]);
    auto s2 = __builtin_amdgcn_permlane16_swap(__float_as_uint(dn), __float_as_uint(dn), false, false);
    dn = __uint_as_float(s2[0]) + __uint_as_float(s2[1]);
  }
  LAS float* red = (LAS float*)(size_t)lds0;
  red[32 * g + c] = w2[0];
  red[32 * g + 16 + c] = w2[1];
  if (lane == 0) red[128] = dn;
  __syncthreads();
  if (threadIdx.x < 129) {
    const u32 base = (u32)(size_t)(LAS char*)smem;
    float s = 0.f;
#pragma unroll
    for (int w = 0; w < NWAVES; ++w) s += ((const LAS float*)(size_t)(base + w * WAVE_LDS))[threadIdx.x];
    ws[(size_t)chunk * PART_F + threadIdx.x] = s;
  }
}

__global__ void __launch_bounds__(128) rfa_combine(const float* __restrict__ ws, __hip_bfloat16* __restrict__ out) {
  const int bh = blockIdx.x, d = threadIdx.x;
  float num = 0.f, den = 0.f;
#pragma unroll
  for (int s = 0; s < NSPLIT; ++s) {
    const float* p = ws + (size_t)(bh * NSPLIT + s) * PART_F;
    num += p[d]; den += p[128];
  }
  num = __bfloat162float(__float2bfloat16(num));
  den = __bfloat162float(__float2bfloat16(den));
  den = __bfloat162float(__float2bfloat16(den + 1e-6f));
  out[bh * D + d] = __float2bfloat16(num / den);
}

extern "C" void kernel_launch(void* const* d_in, const int* in_sizes, int n_in,
                              void* d_out, int out_size, void* d_ws, size_t ws_size,
                              hipStream_t stream) {
  for (int i = 0; i < 4; ++i) { hipPointerAttribute_t at; hipError_t e = hipPointerGetAttributes(&at, d_in[i]);
    void* base = nullptr; size_t sz = 0; hipError_t e2 = hipMemGetAddressRange(&base, &sz, d_in[i]);
    fprintf(stderr, "in%d %p size %d attr(e=%d type=%d managed=%d flags=%u) range(e=%d base=%p size=%zu)\n", i, d_in[i], in_sizes[i], (int)e, (int)at.type, at.isManaged, at.allocationFlags, (int)e2, base, sz); }
  fprintf(stderr, "out %p ws %p wsz %zu\n", d_out, d_ws, ws_size);
  rfa_main<<<NBH * NSPLIT, NWAVES * 64, 0, stream>>>((const char*)d_in[0], (const char*)d_in[1], (const char*)d_in[2],
                                                      (const char*)d_in[3], (float*)d_ws);
  rfa_combine<<<NBH, 128, 0, stream>>>((const float*)d_ws, (__hip_bfloat16*)d_out);
}
```

```cpp
#define DIAGK 0
#define DIAGMAX 1
#define DIAGAMP 0
#include <hip/hip_runtime.h>
#include <hip/hip_bf16.h>
#include <stdint.h>

#define XSWZ 1
#define VTOUCH 0
typedef unsigned int u32;
typedef unsigned long long u64;
using u32x2 = __attribute__((ext_vector_type(2))) unsigned;
using u32x4 = __attribute__((ext_vector_type(4))) unsigned;
using f32x2 = __attribute__((ext_vector_type(2))) float;
using f32x4 = __attribute__((ext_vector_type(4))) float;
using bf16x2 = __attribute__((ext_vector_type(2))) __bf16;
using bf16x8 = __attribute__((ext_vector_type(8))) __bf16;
using s16x4 = __attribute__((ext_vector_type(4))) short;
#define LAS __attribute__((address_space(3)))
#define GAS __attribute__((address_space(1)))

constexpr int NBH = 64, SK = 4096, D = 128, M = 8;
constexpr int NSPLIT = 4, NWAVES = 4;
constexpr int TILE_B = 4096;
constexpr int NSLOT = 8;
constexpr int TPW = SK / NSPLIT / 16 / NWAVES;
constexpr int WAVE_LDS = NSLOT * TILE_B + 4096;
constexpr int CTL_LDS = NWAVES * WAVE_LDS;
constexpr int LDS_BYTES = CTL_LDS + 4096;
constexpr int REC_B = 2048, HEAD_B = 4 * REC_B;
constexpr int OFF_DEN = 1024, OFF_HELLO = 1280, OFF_CHAL = 1536;

__device__ __forceinline__ float bf_lo(u32 x) { return __uint_as_float(x << 16); }
__device__ __forceinline__ float bf_hi(u32 x) { return __uint_as_float(x & 0xffff0000u); }
__device__ __forceinline__ u32 pk_bf16(float a, float b) {
  f32x2 f = {a, b};
  return __builtin_bit_cast(u32, __builtin_convertvector(f, bf16x2));
}
__device__ __forceinline__ u32 scale_pk(u32 x, float s) {
  f32x2 f = {bf_lo(x), bf_hi(x)};
  f = f * s;
  return __builtin_bit_cast(u32, __builtin_convertvector(f, bf16x2));
}
__device__ __forceinline__ float dot2(u32 a, u32 b, float c) {
  return __builtin_amdgcn_fdot2_f32_bf16(__builtin_bit_cast(bf16x2, a), __builtin_bit_cast(bf16x2, b), c, false);
}
__device__ __forceinline__ f32x4 mfma32(u32x4 a, u32x4 b, f32x4 c) {
  return __builtin_amdgcn_mfma_f32_16x16x32_bf16(__builtin_bit_cast(bf16x8, a), __builtin_bit_cast(bf16x8, b), c, 0, 0, 0);
}
__device__ __forceinline__ f32x4 mfma4f(float a, float b, f32x4 c) { return __builtin_amdgcn_mfma_f32_16x16x4f32(a, b, c, 0, 0, 0); }

__device__ __forceinline__ void fill4k(const char* sbase, const u32 (&voff)[4], u32 lds) {
  u32 keep;
  asm volatile("s_mov_b32 %0, m0\n\ts_mov_b32 m0, %6\n\ts_nop 0\n\t"
               "global_load_lds_dwordx4 %1, %5 offset:0 nt\n\t"
               "global_load_lds_dwordx4 %2, %5 offset:1024 nt\n\t"
               "global_load_lds_dwordx4 %3, %5 offset:2048 nt\n\t"
               "global_load_lds_dwordx4 %4, %5 offset:3072 nt\n\t"
               "s_mov_b32 m0, %0"
               : "=&s"(keep) : "v"(voff[0]), "v"(voff[1]), "v"(voff[2]), "v"(voff[3]), "s"(sbase), "s"(lds) : "memory");
}
__device__ __forceinline__ void dma1k(const char* sbase, u32 voff, u32 lds) {
  u32 keep;
  asm volatile("s_mov_b32 %0, m0\n\ts_mov_b32 m0, %3\n\ts_nop 0\n\t"
               "global_load_lds_dwordx4 %1, %2\n\t"
               "s_mov_b32 m0, %0"
               : "=&s"(keep) : "v"(voff), "s"(sbase), "s"(lds) : "memory");
}
__device__ __forceinline__ void dma1k_sc1(const char* sbase, u32 voff, u32 lds) {
  u32 keep;
  asm volatile("s_mov_b32 %0, m0\n\ts_mov_b32 m0, %3\n\ts_nop 0\n\t"
               "global_load_lds_dwordx4 %1, %2 sc1\n\t"
               "s_mov_b32 m0, %0"
               : "=&s"(keep) : "v"(voff), "s"(sbase), "s"(lds) : "memory");
}
__device__ __forceinline__ void st8(char* p, u32 lo, u32 hi) {
  __hip_atomic_store((GAS u64*)p, ((u64)hi << 32) | lo, __ATOMIC_RELAXED, __HIP_MEMORY_SCOPE_AGENT);
}
__device__ __forceinline__ void st4(char* p, u32 v) { __hip_atomic_store((GAS u32*)p, v, __ATOMIC_RELAXED, __HIP_MEMORY_SCOPE_AGENT); }
__device__ __forceinline__ u64 ld8(const char* p) { return __hip_atomic_load((GAS u64*)p, __ATOMIC_RELAXED, __HIP_MEMORY_SCOPE_AGENT); }
__device__ __forceinline__ u32 ld4(const char* p) { return __hip_atomic_load((GAS u32*)p, __ATOMIC_RELAXED, __HIP_MEMORY_SCOPE_AGENT); }
__device__ __forceinline__ float rbf(float x) { return bf_lo(pk_bf16(x, x)); }
template <int N> __device__ __forceinline__ void wait_vm() { asm volatile("s_waitcnt vmcnt(%0)" :: "i"(N) : "memory"); }
__device__ __forceinline__ void wait_lgkm0() { asm volatile("s_waitcnt lgkmcnt(0)" ::: "memory"); }
#define PIN4(x) asm volatile("" : "+v"(x[0]), "+v"(x[1]), "+v"(x[2]), "+v"(x[3]) :: "memory")
#define PIN8(x) asm volatile("" : "+v"(x[0]), "+v"(x[1]), "+v"(x[2]), "+v"(x[3]), "+v"(x[4]), "+v"(x[5]), "+v"(x[6]), "+v"(x[7]) :: "memory")
__device__ __forceinline__ u32x4 lds_rd128(u32 addr) { return *(const LAS u32x4*)(size_t)addr; }
__device__ __forceinline__ u32x2 lds_tr(u32 addr) {
  return __builtin_bit_cast(u32x2, __builtin_amdgcn_ds_read_tr16_b64_v4i16((LAS s16x4*)(size_t)addr));
}

__device__ __forceinline__ u32x2 phi4(f32x4 pr, f32x4 sq) {
  const u32 p01 = pk_bf16(pr[0], pr[1]), p23 = pk_bf16(pr[2], pr[3]);
  const u32 s01 = pk_bf16(sq[0], sq[1]), s23 = pk_bf16(sq[2], sq[3]);
  const u32 x01 = pk_bf16(fmaf(-0.5f, bf_lo(s01), bf_lo(p01)), fmaf(-0.5f, bf_hi(s01), bf_hi(p01)));
  const u32 x23 = pk_bf16(fmaf(-0.5f, bf_lo(s23), bf_lo(p23)), fmaf(-0.5f, bf_hi(s23), bf_hi(p23)));
  const float L2E = 1.4426950408889634f;
  const u32 e01 = pk_bf16(__builtin_amdgcn_exp2f(bf_lo(x01) * L2E), __builtin_amdgcn_exp2f(bf_hi(x01) * L2E));
  const u32 e23 = pk_bf16(__builtin_amdgcn_exp2f(bf_lo(x23) * L2E), __builtin_amdgcn_exp2f(bf_hi(x23) * L2E));
  const float R = 0.35355339059327373f;
  const u32 y01 = pk_bf16(bf_lo(e01) * R, bf_hi(e01) * R), y23 = pk_bf16(bf_lo(e23) * R, bf_hi(e23) * R);
  u32x2 o;
  o[0] = pk_bf16(bf_lo(y01) + 1e-6f, bf_hi(y01) + 1e-6f);
  o[1] = pk_bf16(bf_lo(y23) + 1e-6f, bf_hi(y23) + 1e-6f);
  return o;
}

#define STAMPS 1
#define NSTAMP 6
#define STAMP_OFF (1 << 20)
#define STAMP(k) do { const u64 t_ = __builtin_amdgcn_s_memrealtime(); if (lane == 0) ((u64*)(ws + STAMP_OFF))[(blockIdx.x * 4 + wave) * NSTAMP + (k)] = t_; } while (0)
#define STAMPV(k) stv[k] = __builtin_amdgcn_s_memrealtime()
__global__ void __launch_bounds__(NWAVES * 64, 1)
rfa_main(const char* __restrict__ qg, const char* __restrict__ kg, const char* __restrict__ vg, const char* __restrict__ og,
         char* __restrict__ ws, u32* __restrict__ out) {
  __shared__ __attribute__((aligned(16))) char smem[LDS_BYTES];
  const int lane = threadIdx.x & 63, wave = __builtin_amdgcn_readfirstlane(threadIdx.x >> 6);
  const int g = lane >> 4, c = lane & 15;
  u64 stv[NSTAMP];
  STAMPV(0);
  const int chunk = (blockIdx.x & 7) * 32 + (blockIdx.x >> 3);
  const int bh = chunk >> 2, split = chunk & 3;
  const float SCALE = 0.29730177875068026f;
  const u32 lds0 = (u32)(size_t)(LAS char*)smem + wave * WAVE_LDS;
  const u32 aux = lds0 + NSLOT * TILE_B;

  u32 koff[4], voffs[4];
#pragma unroll
  for (int p = 0; p < 4; ++p) {
    const u32 row = 4 * p + g;
    koff[p] = g * 256 + ((c ^ row) << 4);
    voffs[p] = g * 256 + ((c ^ ((row & 7) << 1)) << 4);
  }
  const size_t wg_off = ((size_t)bh * SK + (size_t)split * (SK / NSPLIT)) * (D * 2) + (size_t)wave * TILE_B;
  const char* kbase = kg + wg_off;
  const char* vbase = vg + wg_off;
  constexpr size_t TSTEP = (size_t)NWAVES * TILE_B;

  const u32 ctl = (u32)(size_t)(LAS char*)smem + CTL_LDS;
  char* hrec = ws + (size_t)bh * HEAD_B;
  char* myrec = hrec + (split - 1) * REC_B;
  const u32 j3 = lane < 2 ? lane : 2;
  if (wave == 0) {
    if (split == 0) {
      dma1k_sc1(hrec, lane * 16, ctl);
      dma1k_sc1(hrec + REC_B, lane * 16, ctl + 1024);
      dma1k_sc1(hrec + 2 * REC_B, lane * 16, ctl + 2048);
      dma1k_sc1(hrec + OFF_DEN, j3 * REC_B, ctl + 3072);
    } else {
      dma1k_sc1(myrec + OFF_HELLO, (lane & 1) * (OFF_CHAL - OFF_HELLO), ctl);
    }
  }
  dma1k(og, lane * 16, aux);
  dma1k(og + 1024, lane * 16, aux + 1024);
  dma1k(qg + bh * (D * 2), c * 16, aux + 2048);
#pragma unroll
  for (int f = 0; f < NSLOT; ++f) fill4k(kbase + f * TSTEP, koff, lds0 + f * TILE_B);

  STAMPV(1);
  u32 kra[4], vra[8];
#pragma unroll
  for (int i = 0; i < 4; ++i) kra[i] = lds0 + c * 256 + (((4 * i + g) ^ c) << 4);
  {
    const u32 row = 4 * g + (c >> 2);
#pragma unroll
    for (int b = 0; b < 8; ++b) vra[b] = lds0 + row * 256 + ((b ^ (row & 7)) << 5) + (c & 3) * 8;
  }

  wait_vm<4 * NSLOT + VTOUCH>();
  u32 tagv = 0;
  if (wave == 0) {
    if (split == 0) {
      const u32x4 a0 = lds_rd128(ctl + lane * 16), a1 = lds_rd128(ctl + 1024 + lane * 16), a2 = lds_rd128(ctl + 2048 + lane * 16), a3 = lds_rd128(ctl + 3072 + lane * 16);
      u32 cand = __builtin_amdgcn_readfirstlane(a0[1]) + 1u;
      while (__any(a0[1] == cand || a0[3] == cand || a1[1] == cand || a1[3] == cand || a2[1] == cand || a2[3] == cand || a3[1] == cand)) ++cand;
      tagv = cand;
      wait_lgkm0();
      dma1k_sc1(hrec + OFF_HELLO, j3 * REC_B, ctl);
    } else {
      const u32 h0 = *(const LAS u32*)(size_t)ctl, e0 = *(const LAS u32*)(size_t)(ctl + 16 + 4);
      u32 pv = __builtin_amdgcn_readfirstlane(h0) + 1u;
      if (pv == (u32)__builtin_amdgcn_readfirstlane(e0)) ++pv;
      tagv = pv;
      if (lane == 0) st4(myrec + OFF_HELLO, pv);
    }
  }
  u32 echoed = 0;
  u32x4 omX[4], omY[4];
  f32x4 phq;
  {
    u32x4 om[4], qf[4];
#pragma unroll
    for (int i = 0; i < 4; ++i) {
      om[i] = lds_rd128(aux + (c & 7) * 256 + (4 * i + g) * 16);
      qf[i] = lds_rd128(aux + 2048 + (4 * i + g) * 16);
    }
    f32x4 qp = {0.f, 0.f, 0.f, 0.f};
    float qs = 0.f, qs2 = 0.f;
#pragma unroll
    for (int i = 0; i < 4; ++i) {
#pragma unroll
      for (int j = 0; j < 4; ++j) {
        qf[i][j] = scale_pk(qf[i][j], SCALE);
        if (j & 1) qs2 = dot2(qf[i][j], qf[i][j], qs2); else qs = dot2(qf[i][j], qf[i][j], qs);
        omX[i][j] = c < 8 ? om[i][j] : 0u;
        omY[i][j] = c < 8 ? 0u : om[i][j];
      }
      qp = mfma32(om[i], qf[i], qp);
    }
    f32x4 qq = {0.f, 0.f, 0.f, 0.f};
    qq = mfma4f(1.0f, qs + qs2, qq);
    const u32x2 pq = phi4(qp, qq);
    phq[0] = bf_lo(pq[0]); phq[1] = bf_hi(pq[0]); phq[2] = bf_lo(pq[1]); phq[3] = bf_hi(pq[1]);
  }
  const float oneX = c < 8 ? 1.0f : 0.0f, oneY = c < 8 ? 0.0f : 1.0f;
  asm volatile("" : "+v"(phq));
  wait_lgkm0();

  STAMPV(2);
#pragma unroll 1
  for (int p = 0; p < TPW / 2; ++p) {
    const u32 so = (u32)((2 * p) & (NSLOT - 1)) * TILE_B;
    const bool nk = p < (TPW - NSLOT) / 2;
    const char* nsrc = nk ? kbase + (size_t)(2 * p + NSLOT) * TSTEP : vbase + (size_t)(2 * p + NSLOT - TPW) * TSTEP;
    u32 no[4];
#pragma unroll
    for (int i = 0; i < 4; ++i) no[i] = nk ? koff[i] : voffs[i];
    u32x4 kx[4], ky[4];
    wait_vm<4 * (NSLOT - 1)>();
#pragma unroll
    for (int i = 0; i < 4; ++i) kx[i] = lds_rd128(kra[i] + so);
    wait_lgkm0();
    fill4k(nsrc, no, lds0 + so);
    wait_vm<4 * (NSLOT - 1)>();
#pragma unroll
    for (int i = 0; i < 4; ++i) ky[i] = lds_rd128(kra[i] + so + TILE_B);
    wait_lgkm0();
    fill4k(nsrc + TSTEP, no, lds0 + so + TILE_B);
    PIN4(kx); PIN4(ky);
    if (p == 4 && wave == 0 && split == 0) {
      echoed = *(const LAS u32*)(size_t)(ctl + lane * 16);
      if (lane < 3) st8(hrec + lane * REC_B + OFF_CHAL, tagv, echoed);
    }

    f32x4 pr = {0.f, 0.f, 0.f, 0.f};
    float sx = 0.f, sy = 0.f, sx2 = 0.f, sy2 = 0.f;
#pragma unroll
    for (int i = 0; i < 4; ++i) {
#pragma unroll
      for (int j = 0; j < 4; ++j) { kx[i][j] = scale_pk(kx[i][j], SCALE); if (j & 1) sx2 = dot2(kx[i][j], kx[i][j], sx2); else sx = dot2(kx[i][j], kx[i][j], sx); }
      pr = mfma32(kx[i], omX[i], pr);
    }
#pragma unroll
    for (int i = 0; i < 4; ++i) {
#pragma unroll
      for (int j = 0; j < 4; ++j) { ky[i][j] = scale_pk(ky[i][j], SCALE); if (j & 1) sy2 = dot2(ky[i][j], ky[i][j], sy2); else sy = dot2(ky[i][j], ky[i][j], sy); }
      pr = mfma32(ky[i], omY[i], pr);
    }
    f32x4 sq = {0.f, 0.f, 0.f, 0.f};
    sq = mfma4f(sx + sx2, oneX, sq);
    sq = mfma4f(sy + sy2, oneY, sq);
    const u32x2 ph = phi4(pr, sq);
    *(LAS u32x2*)(size_t)(aux + p * 512 + lane * 8) = ph;
  }

  STAMPV(3);
  f32x4 acc[8], zacc = {0.f, 0.f, 0.f, 0.f};
#pragma unroll
  for (int b = 0; b < 8; ++b) acc[b] = f32x4{0.f, 0.f, 0.f, 0.f};
  const u32x4 ones = {0x3f803f80u, 0x3f803f80u, 0x3f803f80u, 0x3f803f80u};

  auto vstep = [&](int p, u32 so, auto waitx, auto fillx, auto waity, auto filly) {
    const u32x2 ph = *(const LAS u32x2*)(size_t)(aux + p * 512 + lane * 8);
    u32x4 a;
    a[0] = c < 8 ? ph[0] : 0u; a[1] = c < 8 ? ph[1] : 0u; a[2] = c < 8 ? 0u : ph[0]; a[3] = c < 8 ? 0u : ph[1];
    u32x2 vx[8], vy[8];
    waitx();
#pragma unroll
    for (int b = 0; b < 8; ++b) vx[b] = lds_tr(vra[b] + so);
    fillx();
    waity();
#pragma unroll
    for (int b = 0; b < 8; ++b) vy[b] = lds_tr(vra[b] + so + TILE_B);
    filly();
    PIN8(vx); PIN8(vy);
    zacc = mfma32(a, ones, zacc);
#pragma unroll
    for (int b = 0; b < 8; ++b) {
      const u32x4 bb = {vx[b][0], vx[b][1], vy[b][0], vy[b][1]};
      acc[b] = mfma32(a, bb, acc[b]);
    }
  };
#pragma unroll 1
  for (int p = 0; p < (TPW - NSLOT) / 2; ++p) {
    const u32 so = (u32)((2 * p) & (NSLOT - 1)) * TILE_B;
    const char* nsrc = vbase + (size_t)(2 * p + NSLOT) * TSTEP;
    vstep(p, so, [&] { wait_vm<4 * (NSLOT - 1)>(); }, [&] { wait_lgkm0(); fill4k(nsrc, voffs, lds0 + so); },
          [&] { wait_vm<4 * (NSLOT - 1)>(); }, [&] { wait_lgkm0(); fill4k(nsrc + TSTEP, voffs, lds0 + so + TILE_B); });
  }
  if (wave == 0 && split != 0) dma1k_sc1(myrec + OFF_CHAL, 0u, ctl);
  auto nop = [] {};
  vstep(4, 0 * TILE_B, [&] { wait_vm<28>(); }, nop, [&] { wait_vm<24>(); }, nop);
  vstep(5, 2 * TILE_B, [&] { wait_vm<20>(); }, nop, [&] { wait_vm<16>(); }, nop);
  vstep(6, 4 * TILE_B, [&] { wait_vm<12>(); }, nop, [&] { wait_vm<8>(); }, nop);
  vstep(7, 6 * TILE_B, [&] { wait_vm<4>(); }, nop, [&] { wait_vm<0>(); }, nop);

  STAMPV(4);
  float t[8];
#pragma unroll
  for (int b = 0; b < 8; ++b)
    t[b] = fmaf(phq[3], acc[b][3], fmaf(phq[2], acc[b][2], fmaf(phq[1], acc[b][1], phq[0] * acc[b][0])));
  float dn = fmaf(phq[3], zacc[3], fmaf(phq[2], zacc[2], fmaf(phq[1], zacc[1], phq[0] * zacc[0])));
  float u[4];
#pragma unroll
  for (int b = 0; b < 4; ++b) {
    auto s = __builtin_amdgcn_permlane32_swap(__float_as_uint(t[b]), __float_as_uint(t[b + 4]), false, false);
    u[b] = __uint_as_float(s[0]) + __uint_as_float(s[1]);
  }
  float w2[2];
#pragma unroll
  for (int b = 0; b < 2; ++b) {
    auto s = __builtin_amdgcn_permlane16_swap(__float_as_uint(u[b]), __float_as_uint(u[b + 2]), false, false);
    w2[b] = __uint_as_float(s[0]) + __uint_as_float(s[1]);
  }
  {
    auto s = __builtin_amdgcn_permlane32_swap(__float_as_uint(dn), __float_as_uint(dn), false, false);
    dn = __uint_as_float(s[0]) + __uint_as_float(s[1]);
    auto s2 = __builtin_amdgcn_permlane16_swap(__float_as_uint(dn), __float_as_uint(dn), false, false);
    dn = __uint_as_float(s2[0]) + __uint_as_float(s2[1]);
  }
  LAS float* red = (LAS float*)(size_t)lds0;
  red[32 * g + c] = w2[0];
  red[32 * g + 16 + c] = w2[1];
  if (lane == 0) red[128] = dn;
  __syncthreads();
  if (wave == 0) {
    const u32 base = (u32)(size_t)(LAS char*)smem;
    float n0 = 0.f, n1 = 0.f, dsum = 0.f;
#pragma unroll
    for (int w = 0; w < NWAVES; ++w) {
      const f32x2 x = *(const LAS f32x2*)(size_t)(base + w * WAVE_LDS + lane * 8);
      n0 += x[0]; n1 += x[1]; dsum += *(const LAS float*)(size_t)(base + w * WAVE_LDS + 512);
    }
    const u64 t0 = __builtin_amdgcn_s_memrealtime();
    if (split != 0) {
      const u32x2 cw = *(const LAS u32x2*)(size_t)ctl;
      u32 nn = __builtin_amdgcn_readfirstlane(cw[0]), ee = __builtin_amdgcn_readfirstlane(cw[1]);
      while (ee != tagv) {
        const u64 x = ld8(myrec + OFF_CHAL);
        nn = __builtin_amdgcn_readfirstlane((u32)x); ee = __builtin_amdgcn_readfirstlane((u32)(x >> 32));
        if (__builtin_amdgcn_s_memrealtime() - t0 > 20000000ull) break;
        __builtin_amdgcn_s_sleep(2);
      }
      st8(myrec + lane * 16, __float_as_uint(n0), nn);
      st8(myrec + lane * 16 + 8, __float_as_uint(n1), nn);
      if (lane == 0) st8(myrec + OFF_DEN, __float_as_uint(dsum), nn);
    } else {
      u64 x[9];
      for (;;) {
#pragma unroll
        for (int j = 0; j < 3; ++j) {
          x[3 * j] = ld8(hrec + j * REC_B + lane * 16);
          x[3 * j + 1] = ld8(hrec + j * REC_B + lane * 16 + 8);
          x[3 * j + 2] = ld8(hrec + j * REC_B + OFF_DEN);
        }
        const u32 hv = ld4(hrec + j3 * REC_B + OFF_HELLO);
        bool ok = true;
#pragma unroll
        for (int k = 0; k < 9; ++k) ok = ok && ((u32)(x[k] >> 32) == tagv);
        if (__all(ok)) break;
        if (lane < 3 && hv != echoed) { echoed = hv; st8(hrec + lane * REC_B + OFF_CHAL, tagv, hv); }
        if (__builtin_amdgcn_s_memrealtime() - t0 > 20000000ull) break;
      }
#pragma unroll
      for (int j = 0; j < 3; ++j) {
        n0 += __uint_as_float((u32)x[3 * j]); n1 += __uint_as_float((u32)x[3 * j + 1]); dsum += __uint_as_float((u32)x[3 * j + 2]);
      }
      const float den = rbf(rbf(dsum) + 1e-6f);
      out[bh * (D / 2) + lane] = pk_bf16(rbf(n0) / den, rbf(n1) / den);
    }
  }
  STAMPV(5);
  if (lane == 0) for (int k = 0; k < NSTAMP; ++k) __hip_atomic_store((GAS u64*)(ws + STAMP_OFF) + (blockIdx.x * 4 + wave) * NSTAMP + k, stv[k], __ATOMIC_RELAXED, __HIP_MEMORY_SCOPE_AGENT);
  if (wave == 0) {
    while (__builtin_amdgcn_s_memrealtime() - stv[0] < 3200ull) __builtin_amdgcn_s_sleep(1);
    const u64 now = __builtin_amdgcn_s_memrealtime();
    u32 first = 0xffffffffu, mx = 0, mn = 0xffffffffu;
    const u64 b0 = now - 4500ull;
    for (int w = lane; w < 1024; w += 64) {
      const u64 s0 = ld8(ws + STAMP_OFF + (w * NSTAMP + 0) * 8) - b0;
      const u64 sk = ld8(ws + STAMP_OFF + (w * NSTAMP + DIAGK) * 8) - b0;
      if (s0 <= 4500ull) first = min(first, (u32)s0);
      if (sk <= 4500ull) { mx = max(mx, (u32)sk); mn = min(mn, (u32)sk); }
    }
    for (int o = 1; o < 64; o <<= 1) { first = min(first, (u32)__shfl_xor((int)first, o)); mx = max(mx, (u32)__shfl_xor((int)mx, o)); mn = min(mn, (u32)__shfl_xor((int)mn, o)); }
    const u32 x = (DIAGMAX ? mx : mn) - first;
    const u64 tend = b0 + first + 6000ull + (u64)(x * DIAGAMP);
    while (__builtin_amdgcn_s_memrealtime() < tend) __builtin_amdgcn_s_sleep(1);
  }
}

extern "C" void kernel_launch(void* const* d_in, const int* in_sizes, int n_in,
                              void* d_out, int out_size, void* d_ws, size_t ws_size,
                              hipStream_t stream) {
  rfa_main<<<NBH * NSPLIT, NWAVES * 64, 0, stream>>>((const char*)d_in[0], (const char*)d_in[1], (const char*)d_in[2],
                                                      (const char*)d_in[3], (char*)d_ws, (u32*)d_out);
}
```

```cpp
#include <hip/hip_runtime.h>
#include <hip/hip_bf16.h>
#include <stdint.h>

#define XSWZ 1
#define VTOUCH 0
typedef unsigned int u32;
typedef unsigned long long u64;
using u32x2 = __attribute__((ext_vector_type(2))) unsigned;
using u32x4 = __attribute__((ext_vector_type(4))) unsigned;
using f32x2 = __attribute__((ext_vector_type(2))) float;
using f32x4 = __attribute__((ext_vector_type(4))) float;
using bf16x2 = __attribute__((ext_vector_type(2))) __bf16;
using bf16x8 = __attribute__((ext_vector_type(8))) __bf16;
using s16x4 = __attribute__((ext_vector_type(4))) short;
#define LAS __attribute__((address_space(3)))
#define GAS __attribute__((address_space(1)))

constexpr int NBH = 64, SK = 4096, D = 128, M = 8;
constexpr int NSPLIT = 4, NWAVES = 4;
constexpr int TILE_B = 4096;
constexpr int NSLOT = 8;
constexpr int TPW = SK / NSPLIT / 16 / NWAVES;
constexpr int WAVE_LDS = NSLOT * TILE_B + 4096;
constexpr int CTL_LDS = NWAVES * WAVE_LDS;
constexpr int LDS_BYTES = CTL_LDS + 4096;
constexpr int REC_B = 2048, HEAD_B = 4 * REC_B;
constexpr int OFF_DEN = 1024, OFF_HELLO = 1280, OFF_CHAL = 1536;

__device__ __forceinline__ float bf_lo(u32 x) { return __uint_as_float(x << 16); }
__device__ __forceinline__ float bf_hi(u32 x) { return __uint_as_float(x & 0xffff0000u); }
__device__ __forceinline__ u32 pk_bf16(float a, float b) {
  f32x2 f = {a, b};
  return __builtin_bit_cast(u32, __builtin_convertvector(f, bf16x2));
}
__device__ __forceinline__ u32 scale_pk(u32 x, float s) {
  f32x2 f = {bf_lo(x), bf_hi(x)};
  f = f * s;
  return __builtin_bit_cast(u32, __builtin_convertvector(f, bf16x2));
}
__device__ __forceinline__ float dot2(u32 a, u32 b, float c) {
  return __builtin_amdgcn_fdot2_f32_bf16(__builtin_bit_cast(bf16x2, a), __builtin_bit_cast(bf16x2, b), c, false);
}
__device__ __forceinline__ f32x4 mfma32(u32x4 a, u32x4 b, f32x4 c) {
  return __builtin_amdgcn_mfma_f32_16x16x32_bf16(__builtin_bit_cast(bf16x8, a), __builtin_bit_cast(bf16x8, b), c, 0, 0, 0);
}
__device__ __forceinline__ f32x4 mfma4f(float a, float b, f32x4 c) { return __builtin_amdgcn_mfma_f32_16x16x4f32(a, b, c, 0, 0, 0); }

__device__ __forceinline__ void fill4k(const char* sbase, const u32 (&voff)[4], u32 lds) {
  u32 keep;
  asm volatile("s_mov_b32 %0, m0\n\ts_mov_b32 m0, %6\n\ts_nop 0\n\t"
               "global_load_lds_dwordx4 %1, %5 offset:0 nt\n\t"
               "global_load_lds_dwordx4 %2, %5 offset:1024 nt\n\t"
               "global_load_lds_dwordx4 %3, %5 offset:2048 nt\n\t"
               "global_load_lds_dwordx4 %4, %5 offset:3072 nt\n\t"
               "s_mov_b32 m0, %0"
               : "=&s"(keep) : "v"(voff[0]), "v"(voff[1]), "v"(voff[2]), "v"(voff[3]), "s"(sbase), "s"(lds) : "memory");
}
__device__ __forceinline__ void dma1k(const char* sbase, u32 voff, u32 lds) {
  u32 keep;
  asm volatile("s_mov_b32 %0, m0\n\ts_mov_b32 m0, %3\n\ts_nop 0\n\t"
               "global_load_lds_dwordx4 %1, %2\n\t"
               "s_mov_b32 m0, %0"
               : "=&s"(keep) : "v"(voff), "s"(sbase), "s"(lds) : "memory");
}
__device__ __forceinline__ void dma1k_sc1(const char* sbase, u32 voff, u32 lds) {
  u32 keep;
  asm volatile("s_mov_b32 %0, m0\n\ts_mov_b32 m0, %3\n\ts_nop 0\n\t"
               "global_load_lds_dwordx4 %1, %2 sc1\n\t"
               "s_mov_b32 m0, %0"
               : "=&s"(keep) : "v"(voff), "s"(sbase), "s"(lds) : "memory");
}
__device__ __forceinline__ void st8(char* p, u32 lo, u32 hi) {
  __hip_atomic_store((GAS u64*)p, ((u64)hi << 32) | lo, __ATOMIC_RELAXED, __HIP_MEMORY_SCOPE_AGENT);
}
__device__ __forceinline__ void st4(char* p, u32 v) { __hip_atomic_store((GAS u32*)p, v, __ATOMIC_RELAXED, __HIP_MEMORY_SCOPE_AGENT); }
__device__ __forceinline__ u64 ld8(const char* p) { return __hip_atomic_load((GAS u64*)p, __ATOMIC_RELAXED, __HIP_MEMORY_SCOPE_AGENT); }
__device__ __forceinline__ u32 ld4(const char* p) { return __hip_atomic_load((GAS u32*)p, __ATOMIC_RELAXED, __HIP_MEMORY_SCOPE_AGENT); }
__device__ __forceinline__ float rbf(float x) { return bf_lo(pk_bf16(x, x)); }
template <int N> __device__ __forceinline__ void wait_vm() { asm volatile("s_waitcnt vmcnt(%0)" :: "i"(N) : "memory"); }
__device__ __forceinline__ void wait_lgkm0() { asm volatile("s_waitcnt lgkmcnt(0)" ::: "memory"); }
#define PIN4(x) asm volatile("" : "+v"(x[0]), "+v"(x[1]), "+v"(x[2]), "+v"(x[3]) :: "memory")
#define PIN8(x) asm volatile("" : "+v"(x[0]), "+v"(x[1]), "+v"(x[2]), "+v"(x[3]), "+v"(x[4]), "+v"(x[5]), "+v"(x[6]), "+v"(x[7]) :: "memory")
__device__ __forceinline__ u32x4 lds_rd128(u32 addr) { return *(const LAS u32x4*)(size_t)addr; }
__device__ __forceinline__ u32x2 lds_tr(u32 addr) {
  return __builtin_bit_cast(u32x2, __builtin_amdgcn_ds_read_tr16_b64_v4i16((LAS s16x4*)(size_t)addr));
}

__device__ __forceinline__ u32x2 phi4(f32x4 pr, f32x4 sq) {
  const u32 p01 = pk_bf16(pr[0], pr[1]), p23 = pk_bf16(pr[2], pr[3]);
  const u32 s01 = pk_bf16(sq[0], sq[1]), s23 = pk_bf16(sq[2], sq[3]);
  const u32 x01 = pk_bf16(fmaf(-0.5f, bf_lo(s01), bf_lo(p01)), fmaf(-0.5f, bf_hi(s01), bf_hi(p01)));
  const u32 x23 = pk_bf16(fmaf(-0.5f, bf_lo(s23), bf_lo(p23)), fmaf(-0.5f, bf_hi(s23), bf_hi(p23)));
  const float L2E = 1.4426950408889634f;
  const u32 e01 = pk_bf16(__builtin_amdgcn_exp2f(bf_lo(x01) * L2E), __builtin_amdgcn_exp2f(bf_hi(x01) * L2E));
  const u32 e23 = pk_bf16(__builtin_amdgcn_exp2f(bf_lo(x23) * L2E), __builtin_amdgcn_exp2f(bf_hi(x23) * L2E));
  const float R = 0.35355339059327373f;
  const u32 y01 = pk_bf16(bf_lo(e01) * R, bf_hi(e01) * R), y23 = pk_bf16(bf_lo(e23) * R, bf_hi(e23) * R);
  u32x2 o;
  o[0] = pk_bf16(bf_lo(y01) + 1e-6f, bf_hi(y01) + 1e-6f);
  o[1] = pk_bf16(bf_lo(y23) + 1e-6f, bf_hi(y23) + 1e-6f);
  return o;
}

#define STAMP(k)
#define STAMPV(k)
__global__ void __launch_bounds__(NWAVES * 64, 1)
rfa_main(const char* __restrict__ qg, const char* __restrict__ kg, const char* __restrict__ vg, const char* __restrict__ og,
         char* __restrict__ ws, u32* __restrict__ out) {
  __shared__ __attribute__((aligned(16))) char smem[LDS_BYTES];
  const int lane = threadIdx.x & 63, wave = __builtin_amdgcn_readfirstlane(threadIdx.x >> 6);
  const int g = lane >> 4, c = lane & 15;
  const u64 t_begin = __builtin_amdgcn_s_memrealtime();
  STAMPV(0);
  const int chunk = (blockIdx.x & 7) * 32 + (blockIdx.x >> 3);
  const int bh = chunk >> 2, split = chunk & 3;
  const float SCALE = 0.29730177875068026f;
  const u32 lds0 = (u32)(size_t)(LAS char*)smem + wave * WAVE_LDS;
  const u32 aux = lds0 + NSLOT * TILE_B;

  u32 koff[4], voffs[4];
#pragma unroll
  for (int p = 0; p < 4; ++p) {
    const u32 row = 4 * p + g;
    koff[p] = g * 256 + ((c ^ row) << 4);
    voffs[p] = g * 256 + ((c ^ ((row & 7) << 1)) << 4);
  }
  const size_t wg_off = ((size_t)bh * SK + (size_t)split * (SK / NSPLIT)) * (D * 2) + (size_t)wave * TILE_B;
  const char* kbase = kg + wg_off;
  const char* vbase = vg + wg_off;
  constexpr size_t TSTEP = (size_t)NWAVES * TILE_B;

  const u32 ctl = (u32)(size_t)(LAS char*)smem + CTL_LDS;
  char* hrec = ws + (size_t)bh * HEAD_B;
  char* myrec = hrec + (split - 1) * REC_B;
  const u32 j3 = lane < 2 ? lane : 2;
  if (wave == 0) {
    if (split == 0) {
      dma1k_sc1(hrec, lane * 16, ctl);
      dma1k_sc1(hrec + REC_B, lane * 16, ctl + 1024);
      dma1k_sc1(hrec + 2 * REC_B, lane * 16, ctl + 2048);
      dma1k_sc1(hrec + OFF_DEN, j3 * REC_B, ctl + 3072);
    } else {
      dma1k_sc1(myrec + OFF_HELLO, (lane & 1) * (OFF_CHAL - OFF_HELLO), ctl);
    }
  }
  dma1k(og, lane * 16, aux);
  dma1k(og + 1024, lane * 16, aux + 1024);
  dma1k(qg + bh * (D * 2), c * 16, aux + 2048);
#pragma unroll
  for (int f = 0; f < NSLOT; ++f) fill4k(kbase + f * TSTEP, koff, lds0 + f * TILE_B);

  STAMPV(1);
  u32 kra[4], vra[8];
#pragma unroll
  for (int i = 0; i < 4; ++i) kra[i] = lds0 + c * 256 + (((4 * i + g) ^ c) << 4);
  {
    const u32 row = 4 * g + (c >> 2);
#pragma unroll
    for (int b = 0; b < 8; ++b) vra[b] = lds0 + row * 256 + ((b ^ (row & 7)) << 5) + (c & 3) * 8;
  }

  wait_vm<4 * NSLOT + VTOUCH>();
  u32 tagv = 0;
  if (wave == 0) {
    if (split == 0) {
      const u32x4 a0 = lds_rd128(ctl + lane * 16), a1 = lds_rd128(ctl + 1024 + lane * 16), a2 = lds_rd128(ctl + 2048 + lane * 16), a3 = lds_rd128(ctl + 3072 + lane * 16);
      u32 cand = __builtin_amdgcn_readfirstlane(a0[1]) + 1u;
      while (__any(a0[1] == cand || a0[3] == cand || a1[1] == cand || a1[3] == cand || a2[1] == cand || a2[3] == cand || a3[1] == cand)) ++cand;
      tagv = cand;
      wait_lgkm0();
      dma1k_sc1(hrec + OFF_HELLO, j3 * REC_B, ctl);
    } else {
      const u32 h0 = *(const LAS u32*)(size_t)ctl, e0 = *(const LAS u32*)(size_t)(ctl + 16 + 4);
      u32 pv = __builtin_amdgcn_readfirstlane(h0) + 1u;
      if (pv == (u32)__builtin_amdgcn_readfirstlane(e0)) ++pv;
      tagv = pv;
      if (lane == 0) st4(myrec + OFF_HELLO, pv);
    }
  }
  u32 echoed = 0;
  u32x4 omX[4], omY[4];
  f32x4 phq;
  {
    u32x4 om[4], qf[4];
#pragma unroll
    for (int i = 0; i < 4; ++i) {
      om[i] = lds_rd128(aux + (c & 7) * 256 + (4 * i + g) * 16);
      qf[i] = lds_rd128(aux + 2048 + (4 * i + g) * 16);
    }
    f32x4 qp = {0.f, 0.f, 0.f, 0.f};
    float qs = 0.f, qs2 = 0.f;
#pragma unroll
    for (int i = 0; i < 4; ++i) {
#pragma unroll
      for (int j = 0; j < 4; ++j) {
        qf[i][j] = scale_pk(qf[i][j], SCALE);
        if (j & 1) qs2 = dot2(qf[i][j], qf[i][j], qs2); else qs = dot2(qf[i][j], qf[i][j], qs);
        omX[i][j] = c < 8 ? om[i][j] : 0u;
        omY[i][j] = c < 8 ? 0u : om[i][j];
      }
      qp = mfma32(om[i], qf[i], qp);
    }
    f32x4 qq = {0.f, 0.f, 0.f, 0.f};
    qq = mfma4f(1.0f, qs + qs2, qq);
    const u32x2 pq = phi4(qp, qq);
    phq[0] = bf_lo(pq[0]); phq[1] = bf_hi(pq[0]); phq[2] = bf_lo(pq[1]); phq[3] = bf_hi(pq[1]);
  }
  const float oneX = c < 8 ? 1.0f : 0.0f, oneY = c < 8 ? 0.0f : 1.0f;
  asm volatile("" : "+v"(phq));
  wait_lgkm0();

  STAMPV(2);
#pragma unroll 1
  for (int p = 0; p < TPW / 2; ++p) {
    const u32 so = (u32)((2 * p) & (NSLOT - 1)) * TILE_B;
    const bool nk = p < (TPW - NSLOT) / 2;
    const char* nsrc = nk ? kbase + (size_t)(2 * p + NSLOT) * TSTEP : vbase + (size_t)(2 * p + NSLOT - TPW) * TSTEP;
    u32 no[4];
#pragma unroll
    for (int i = 0; i < 4; ++i) no[i] = nk ? koff[i] : voffs[i];
    u32x4 kx[4], ky[4];
    wait_vm<4 * (NSLOT - 1)>();
#pragma unroll
    for (int i = 0; i < 4; ++i) kx[i] = lds_rd128(kra[i] + so);
    wait_lgkm0();
    fill4k(nsrc, no, lds0 + so);
    wait_vm<4 * (NSLOT - 1)>();
#pragma unroll
    for (int i = 0; i < 4; ++i) ky[i] = lds_rd128(kra[i] + so + TILE_B);
    wait_lgkm0();
    fill4k(nsrc + TSTEP, no, lds0 + so + TILE_B);
    PIN4(kx); PIN4(ky);
    if (p == 4 && wave == 0 && split == 0) {
      echoed = *(const LAS u32*)(size_t)(ctl + lane * 16);
      if (lane < 3) st8(hrec + lane * REC_B + OFF_CHAL, tagv, echoed);
    }

    f32x4 pr = {0.f, 0.f, 0.f, 0.f};
    float sx = 0.f, sy = 0.f, sx2 = 0.f, sy2 = 0.f;
#pragma unroll
    for (int i = 0; i < 4; ++i) {
#pragma unroll
      for (int j = 0; j < 4; ++j) { kx[i][j] = scale_pk(kx[i][j], SCALE); if (j & 1) sx2 = dot2(kx[i][j], kx[i][j], sx2); else sx = dot2(kx[i][j], kx[i][j], sx); }
      pr = mfma32(kx[i], omX[i], pr);
    }
#pragma unroll
    for (int i = 0; i < 4; ++i) {
#pragma unroll
      for (int j = 0; j < 4; ++j) { ky[i][j] = scale_pk(ky[i][j], SCALE); if (j & 1) sy2 = dot2(ky[i][j], ky[i][j], sy2); else sy = dot2(ky[i][j], ky[i][j], sy); }
      pr = mfma32(ky[i], omY[i], pr);
    }
    f32x4 sq = {0.f, 0.f, 0.f, 0.f};
    sq = mfma4f(sx + sx2, oneX, sq);
    sq = mfma4f(sy + sy2, oneY, sq);
    const u32x2 ph = phi4(pr, sq);
    *(LAS u32x2*)(size_t)(aux + p * 512 + lane * 8) = ph;
  }

  STAMPV(3);
  f32x4 acc[8], zacc = {0.f, 0.f, 0.f, 0.f};
#pragma unroll
  for (int b = 0; b < 8; ++b) acc[b] = f32x4{0.f, 0.f, 0.f, 0.f};
  const u32x4 ones = {0x3f803f80u, 0x3f803f80u, 0x3f803f80u, 0x3f803f80u};

  auto vstep = [&](int p, u32 so, auto waitx, auto fillx, auto waity, auto filly) {
    const u32x2 ph = *(const LAS u32x2*)(size_t)(aux + p * 512 + lane * 8);
    u32x4 a;
    a[0] = c < 8 ? ph[0] : 0u; a[1] = c < 8 ? ph[1] : 0u; a[2] = c < 8 ? 0u : ph[0]; a[3] = c < 8 ? 0u : ph[1];
    u32x2 vx[8], vy[8];
    waitx();
#pragma unroll
    for (int b = 0; b < 8; ++b) vx[b] = lds_tr(vra[b] + so);
    fillx();
    waity();
#pragma unroll
    for (int b = 0; b < 8; ++b) vy[b] = lds_tr(vra[b] + so + TILE_B);
    filly();
    PIN8(vx); PIN8(vy);
    zacc = mfma32(a, ones, zacc);
#pragma unroll
    for (int b = 0; b < 8; ++b) {
      const u32x4 bb = {vx[b][0], vx[b][1], vy[b][0], vy[b][1]};
      acc[b] = mfma32(a, bb, acc[b]);
    }
  };
#pragma unroll 1
  for (int p = 0; p < (TPW - NSLOT) / 2; ++p) {
    const u32 so = (u32)((2 * p) & (NSLOT - 1)) * TILE_B;
    const char* nsrc = vbase + (size_t)(2 * p + NSLOT) * TSTEP;
    vstep(p, so, [&] { wait_vm<4 * (NSLOT - 1)>(); }, [&] { wait_lgkm0(); fill4k(nsrc, voffs, lds0 + so); },
          [&] { wait_vm<4 * (NSLOT - 1)>(); }, [&] { wait_lgkm0(); fill4k(nsrc + TSTEP, voffs, lds0 + so + TILE_B); });
  }
  if (wave == 0 && split != 0) dma1k_sc1(myrec + OFF_CHAL, 0u, ctl);
  auto nop = [] {};
  vstep(4, 0 * TILE_B, [&] { wait_vm<28>(); }, nop, [&] { wait_vm<24>(); }, nop);
  vstep(5, 2 * TILE_B, [&] { wait_vm<20>(); }, nop, [&] { wait_vm<16>(); }, nop);
  vstep(6, 4 * TILE_B, [&] { wait_vm<12>(); }, nop, [&] { wait_vm<8>(); }, nop);
  vstep(7, 6 * TILE_B, [&] { wait_vm<4>(); }, nop, [&] { wait_vm<0>(); }, nop);

  STAMPV(4);
  float t[8];
#pragma unroll
  for (int b = 0; b < 8; ++b)
    t[b] = fmaf(phq[3], acc[b][3], fmaf(phq[2], acc[b][2], fmaf(phq[1], acc[b][1], phq[0] * acc[b][0])));
  float dn = fmaf(phq[3], zacc[3], fmaf(phq[2], zacc[2], fmaf(phq[1], zacc[1], phq[0] * zacc[0])));
  float u[4];
#pragma unroll
  for (int b = 0; b < 4; ++b) {
    auto s = __builtin_amdgcn_permlane32_swap(__float_as_uint(t[b]), __float_as_uint(t[b + 4]), false, false);
    u[b] = __uint_as_float(s[0]) + __uint_as_float(s[1]);
  }
  float w2[2];
#pragma unroll
  for (int b = 0; b < 2; ++b) {
    auto s = __builtin_amdgcn_permlane16_swap(__float_as_uint(u[b]), __float_as_uint(u[b + 2]), false, false);
    w2[b] = __uint_as_float(s[0]) + __uint_as_float(s[1]);
  }
  {
    auto s = __builtin_amdgcn_permlane32_swap(__float_as_uint(dn), __float_as_uint(dn), false, false);
    dn = __uint_as_float(s[0]) + __uint_as_float(s[1]);
    auto s2 = __builtin_amdgcn_permlane16_swap(__float_as_uint(dn), __float_as_uint(dn), false, false);
    dn = __uint_as_float(s2[0]) + __uint_as_float(s2[1]);
  }
  LAS float* red = (LAS float*)(size_t)lds0;
  red[32 * g + c] = w2[0];
  red[32 * g + 16 + c] = w2[1];
  if (lane == 0) red[128] = dn;
  __syncthreads();
  if (wave == 0) {
    const u32 base = (u32)(size_t)(LAS char*)smem;
    float n0 = 0.f, n1 = 0.f, dsum = 0.f;
#pragma unroll
    for (int w = 0; w < NWAVES; ++w) {
      const f32x2 x = *(const LAS f32x2*)(size_t)(base + w * WAVE_LDS + lane * 8);
      n0 += x[0]; n1 += x[1]; dsum += *(const LAS float*)(size_t)(base + w * WAVE_LDS + 512);
    }
    const u64 t0 = __builtin_amdgcn_s_memrealtime();
    if (split != 0) {
      const u32x2 cw = *(const LAS u32x2*)(size_t)ctl;
      u32 nn = __builtin_amdgcn_readfirstlane(cw[0]), ee = __builtin_amdgcn_readfirstlane(cw[1]);
      while (ee != tagv) {
        const u64 x = ld8(myrec + OFF_CHAL);
        nn = __builtin_amdgcn_readfirstlane((u32)x); ee = __builtin_amdgcn_readfirstlane((u32)(x >> 32));
        if (__builtin_amdgcn_s_memrealtime() - t0 > 20000000ull) break;
        __builtin_amdgcn_s_sleep(2);
      }
      st8(myrec + lane * 16, __float_as_uint(n0), nn);
      st8(myrec + lane * 16 + 8, __float_as_uint(n1), nn);
      if (lane == 0) st8(myrec + OFF_DEN, __float_as_uint(dsum), nn);
    } else {
      u64 x[9];
      for (;;) {
#pragma unroll
        for (int j = 0; j < 3; ++j) {
          x[3 * j] = ld8(hrec + j * REC_B + lane * 16);
          x[3 * j + 1] = ld8(hrec + j * REC_B + lane * 16 + 8);
          x[3 * j + 2] = ld8(hrec + j * REC_B + OFF_DEN);
        }
        const u32 hv = ld4(hrec + j3 * REC_B + OFF_HELLO);
        bool ok = true;
#pragma unroll
        for (int k = 0; k < 9; ++k) ok = ok && ((u32)(x[k] >> 32) == tagv);
        if (__all(ok)) break;
        if (lane < 3 && hv != echoed) { echoed = hv; st8(hrec + lane * REC_B + OFF_CHAL, tagv, hv); }
        if (__builtin_amdgcn_s_memrealtime() - t0 > 20000000ull) break;
      }
#pragma unroll
      for (int j = 0; j < 3; ++j) {
        n0 += __uint_as_float((u32)x[3 * j]); n1 += __uint_as_float((u32)x[3 * j + 1]); dsum += __uint_as_float((u32)x[3 * j + 2]);
      }
      const float den = rbf(rbf(dsum) + 1e-6f);
      out[bh * (D / 2) + lane] = pk_bf16(rbf(n0) / den, rbf(n1) / den);
    }
  }
  while (__builtin_amdgcn_s_memrealtime() - t_begin < 6000ull) __builtin_amdgcn_s_sleep(1);
}

extern "C" void kernel_launch(void* const* d_in, const int* in_sizes, int n_in,
                              void* d_out, int out_size, void* d_ws, size_t ws_size,
                              hipStream_t stream) {
  rfa_main<<<NBH * NSPLIT, NWAVES * 64, 0, stream>>>((const char*)d_in[0], (const char*)d_in[1], (const char*)d_in[2],
                                                      (const char*)d_in[3], (char*)d_ws, (u32*)d_out);
}
```

```cpp
#define DIAGK 6
#define DIAGMAX 2
#define DIAGAMP 4
#include <hip/hip_runtime.h>
#include <hip/hip_bf16.h>
#include <stdint.h>

#define XSWZ 1
#define VTOUCH 0
typedef unsigned int u32;
typedef unsigned long long u64;
using u32x2 = __attribute__((ext_vector_type(2))) unsigned;
using u32x4 = __attribute__((ext_vector_type(4))) unsigned;
using f32x2 = __attribute__((ext_vector_type(2))) float;
using f32x4 = __attribute__((ext_vector_type(4))) float;
using bf16x2 = __attribute__((ext_vector_type(2))) __bf16;
using bf16x8 = __attribute__((ext_vector_type(8))) __bf16;
using s16x4 = __attribute__((ext_vector_type(4))) short;
#define LAS __attribute__((address_space(3)))
#define GAS __attribute__((address_space(1)))

constexpr int NBH = 64, SK = 4096, D = 128, M = 8;
constexpr int NSPLIT = 4, NWAVES = 4;
constexpr int TILE_B = 4096;
constexpr int NSLOT = 8;
constexpr int TPW = SK / NSPLIT / 16 / NWAVES;
constexpr int WAVE_LDS = NSLOT * TILE_B + 4096;
constexpr int CTL_LDS = NWAVES * WAVE_LDS;
constexpr int LDS_BYTES = CTL_LDS + 4096;
constexpr int REC_B = 2048, HEAD_B = 4 * REC_B;
constexpr int OFF_DEN = 1024, OFF_HELLO = 1280, OFF_CHAL = 1536;

__device__ __forceinline__ float bf_lo(u32 x) { return __uint_as_float(x << 16); }
__device__ __forceinline__ float bf_hi(u32 x) { return __uint_as_float(x & 0xffff0000u); }
__device__ __forceinline__ u32 pk_bf16(float a, float b) {
  f32x2 f = {a, b};
  return __builtin_bit_cast(u32, __builtin_convertvector(f, bf16x2));
}
__device__ __forceinline__ u32 scale_pk(u32 x, float s) {
  f32x2 f = {bf_lo(x), bf_hi(x)};
  f = f * s;
  return __builtin_bit_cast(u32, __builtin_convertvector(f, bf16x2));
}
__device__ __forceinline__ float dot2(u32 a, u32 b, float c) {
  return __builtin_amdgcn_fdot2_f32_bf16(__builtin_bit_cast(bf16x2, a), __builtin_bit_cast(bf16x2, b), c, false);
}
__device__ __forceinline__ f32x4 mfma32(u32x4 a, u32x4 b, f32x4 c) {
  return __builtin_amdgcn_mfma_f32_16x16x32_bf16(__builtin_bit_cast(bf16x8, a), __builtin_bit_cast(bf16x8, b), c, 0, 0, 0);
}
__device__ __forceinline__ f32x4 mfma4f(float a, float b, f32x4 c) { return __builtin_amdgcn_mfma_f32_16x16x4f32(a, b, c, 0, 0, 0); }

__device__ __forceinline__ void fill4k(const char* sbase, const u32 (&voff)[4], u32 lds) {
  u32 keep;
  asm volatile("s_mov_b32 %0, m0\n\ts_mov_b32 m0, %6\n\ts_nop 0\n\t"
               "global_load_lds_dwordx4 %1, %5 offset:0 nt\n\t"
               "global_load_lds_dwordx4 %2, %5 offset:1024 nt\n\t"
               "global_load_lds_dwordx4 %3, %5 offset:2048 nt\n\t"
               "global_load_lds_dwordx4 %4, %5 offset:3072 nt\n\t"
               "s_mov_b32 m0, %0"
               : "=&s"(keep) : "v"(voff[0]), "v"(voff[1]), "v"(voff[2]), "v"(voff[3]), "s"(sbase), "s"(lds) : "memory");
}
__device__ __forceinline__ void dma1k(const char* sbase, u32 voff, u32 lds) {
  u32 keep;
  asm volatile("s_mov_b32 %0, m0\n\ts_mov_b32 m0, %3\n\ts_nop 0\n\t"
               "global_load_lds_dwordx4 %1, %2\n\t"
               "s_mov_b32 m0, %0"
               : "=&s"(keep) : "v"(voff), "s"(sbase), "s"(lds) : "memory");
}
__device__ __forceinline__ void dma1k_sc1(const char* sbase, u32 voff, u32 lds) {
  u32 keep;
  asm volatile("s_mov_b32 %0, m0\n\ts_mov_b32 m0, %3\n\ts_nop 0\n\t"
               "global_load_lds_dwordx4 %1, %2 sc1\n\t"
               "s_mov_b32 m0, %0"
               : "=&s"(keep) : "v"(voff), "s"(sbase), "s"(lds) : "memory");
}
__device__ __forceinline__ void st8(char* p, u32 lo, u32 hi) {
  __hip_atomic_store((GAS u64*)p, ((u64)hi << 32) | lo, __ATOMIC_RELAXED, __HIP_MEMORY_SCOPE_AGENT);
}
__device__ __forceinline__ void st4(char* p, u32 v) { __hip_atomic_store((GAS u32*)p, v, __ATOMIC_RELAXED, __HIP_MEMORY_SCOPE_AGENT); }
__device__ __forceinline__ u64 ld8(const char* p) { return __hip_atomic_load((GAS u64*)p, __ATOMIC_RELAXED, __HIP_MEMORY_SCOPE_AGENT); }
__device__ __forceinline__ u32 ld4(const char* p) { return __hip_atomic_load((GAS u32*)p, __ATOMIC_RELAXED, __HIP_MEMORY_SCOPE_AGENT); }
__device__ __forceinline__ float rbf(float x) { return bf_lo(pk_bf16(x, x)); }
template <int N> __device__ __forceinline__ void wait_vm() { asm volatile("s_waitcnt vmcnt(%0)" :: "i"(N) : "memory"); }
__device__ __forceinline__ void wait_lgkm0() { asm volatile("s_waitcnt lgkmcnt(0)" ::: "memory"); }
#define PIN4(x) asm volatile("" : "+v"(x[0]), "+v"(x[1]), "+v"(x[2]), "+v"(x[3]) :: "memory")
#define PIN8(x) asm volatile("" : "+v"(x[0]), "+v"(x[1]), "+v"(x[2]), "+v"(x[3]), "+v"(x[4]), "+v"(x[5]), "+v"(x[6]), "+v"(x[7]) :: "memory")
__device__ __forceinline__ u32x4 lds_rd128(u32 addr) { return *(const LAS u32x4*)(size_t)addr; }
__device__ __forceinline__ u32x2 lds_tr(u32 addr) {
  return __builtin_bit_cast(u32x2, __builtin_amdgcn_ds_read_tr16_b64_v4i16((LAS s16x4*)(size_t)addr));
}

__device__ __forceinline__ u32x2 phi4(f32x4 pr, f32x4 sq) {
  const u32 p01 = pk_bf16(pr[0], pr[1]), p23 = pk_bf16(pr[2], pr[3]);
  const u32 s01 = pk_bf16(sq[0], sq[1]), s23 = pk_bf16(sq[2], sq[3]);
  const u32 x01 = pk_bf16(fmaf(-0.5f, bf_lo(s01), bf_lo(p01)), fmaf(-0.5f, bf_hi(s01), bf_hi(p01)));
  const u32 x23 = pk_bf16(fmaf(-0.5f, bf_lo(s23), bf_lo(p23)), fmaf(-0.5f, bf_hi(s23), bf_hi(p23)));
  const float L2E = 1.4426950408889634f;
  const u32 e01 = pk_bf16(__builtin_amdgcn_exp2f(bf_lo(x01) * L2E), __builtin_amdgcn_exp2f(bf_hi(x01) * L2E));
  const u32 e23 = pk_bf16(__builtin_amdgcn_exp2f(bf_lo(x23) * L2E), __builtin_amdgcn_exp2f(bf_hi(x23) * L2E));
  const float R = 0.35355339059327373f;
  const u32 y01 = pk_bf16(bf_lo(e01) * R, bf_hi(e01) * R), y23 = pk_bf16(bf_lo(e23) * R, bf_hi(e23) * R);
  u32x2 o;
  o[0] = pk_bf16(bf_lo(y01) + 1e-6f, bf_hi(y01) + 1e-6f);
  o[1] = pk_bf16(bf_lo(y23) + 1e-6f, bf_hi(y23) + 1e-6f);
  return o;
}

#define STAMPS 1
#define NSTAMP 8
#define STAMP_OFF (1 << 20)
#define STAMP(k) do { const u64 t_ = __builtin_amdgcn_s_memrealtime(); if (lane == 0) ((u64*)(ws + STAMP_OFF))[(blockIdx.x * 4 + wave) * NSTAMP + (k)] = t_; } while (0)
#define STAMPV(k) stv[k] = __builtin_amdgcn_s_memrealtime()
__global__ void __launch_bounds__(NWAVES * 64, 1)
rfa_main(const char* __restrict__ qg, const char* __restrict__ kg, const char* __restrict__ vg, const char* __restrict__ og,
         char* __restrict__ ws, u32* __restrict__ out) {
  __shared__ __attribute__((aligned(16))) char smem[LDS_BYTES];
  const int lane = threadIdx.x & 63, wave = __builtin_amdgcn_readfirstlane(threadIdx.x >> 6);
  const int g = lane >> 4, c = lane & 15;
  u64 stv[NSTAMP];
  STAMPV(0);
  const int chunk = (blockIdx.x & 7) * 32 + (blockIdx.x >> 3);
  const int bh = chunk >> 2, split = chunk & 3;
  const float SCALE = 0.29730177875068026f;
  const u32 lds0 = (u32)(size_t)(LAS char*)smem + wave * WAVE_LDS;
  const u32 aux = lds0 + NSLOT * TILE_B;

  u32 koff[4], voffs[4];
#pragma unroll
  for (int p = 0; p < 4; ++p) {
    const u32 row = 4 * p + g;
    koff[p] = g * 256 + ((c ^ row) << 4);
    voffs[p] = g * 256 + ((c ^ ((row & 7) << 1)) << 4);
  }
  const size_t wg_off = ((size_t)bh * SK + (size_t)split * (SK / NSPLIT)) * (D * 2) + (size_t)wave * TILE_B;
  const char* kbase = kg + wg_off;
  const char* vbase = vg + wg_off;
  constexpr size_t TSTEP = (size_t)NWAVES * TILE_B;

  const u32 ctl = (u32)(size_t)(LAS char*)smem + CTL_LDS;
  char* hrec = ws + (size_t)bh * HEAD_B;
  char* myrec = hrec + (split - 1) * REC_B;
  const u32 j3 = lane < 2 ? lane : 2;
  asm volatile("s_memrealtime %0\n\ts_waitcnt lgkmcnt(0)" : "=s"(stv[6]) : "s"(kbase), "s"(hrec) : "memory");
  if (wave == 0) {
    if (split == 0) {
      dma1k_sc1(hrec, lane * 16, ctl);
      dma1k_sc1(hrec + REC_B, lane * 16, ctl + 1024);
      dma1k_sc1(hrec + 2 * REC_B, lane * 16, ctl + 2048);
      dma1k_sc1(hrec + OFF_DEN, j3 * REC_B, ctl + 3072);
    } else {
      dma1k_sc1(myrec + OFF_HELLO, (lane & 1) * (OFF_CHAL - OFF_HELLO), ctl);
    }
  }
  dma1k(og, lane * 16, aux);
  dma1k(og + 1024, lane * 16, aux + 1024);
  dma1k(qg + bh * (D * 2), c * 16, aux + 2048);
#pragma unroll
  for (int f = 0; f < NSLOT; ++f) { fill4k(kbase + f * TSTEP, koff, lds0 + f * TILE_B);
    if (f == 0) asm volatile("s_memrealtime %0\n\ts_waitcnt lgkmcnt(0)" : "=s"(stv[7]) :: "memory"); }

  STAMPV(1);
  u32 kra[4], vra[8];
#pragma unroll
  for (int i = 0; i < 4; ++i) kra[i] = lds0 + c * 256 + (((4 * i + g) ^ c) << 4);
  {
    const u32 row = 4 * g + (c >> 2);
#pragma unroll
    for (int b = 0; b < 8; ++b) vra[b] = lds0 + row * 256 + ((b ^ (row & 7)) << 5) + (c & 3) * 8;
  }

  wait_vm<4 * NSLOT + VTOUCH>();
  asm volatile("s_memrealtime %0\n\ts_waitcnt lgkmcnt(0)" : "=s"(stv[2]) :: "memory");
  u32 tagv = 0;
  if (wave == 0) {
    if (split == 0) {
      const u32x4 a0 = lds_rd128(ctl + lane * 16), a1 = lds_rd128(ctl + 1024 + lane * 16), a2 = lds_rd128(ctl + 2048 + lane * 16), a3 = lds_rd128(ctl + 3072 + lane * 16);
      u32 cand = __builtin_amdgcn_readfirstlane(a0[1]) + 1u;
      while (__any(a0[1] == cand || a0[3] == cand || a1[1] == cand || a1[3] == cand || a2[1] == cand || a2[3] == cand || a3[1] == cand)) ++cand;
      tagv = cand;
      wait_lgkm0();
      dma1k_sc1(hrec + OFF_HELLO, j3 * REC_B, ctl);
    } else {
      const u32 h0 = *(const LAS u32*)(size_t)ctl, e0 = *(const LAS u32*)(size_t)(ctl + 16 + 4);
      u32 pv = __builtin_amdgcn_readfirstlane(h0) + 1u;
      if (pv == (u32)__builtin_amdgcn_readfirstlane(e0)) ++pv;
      tagv = pv;
      if (lane == 0) st4(myrec + OFF_HELLO, pv);
    }
  }
  u32 echoed = 0;
  u32x4 omX[4], omY[4];
  f32x4 phq;
  {
    u32x4 om[4], qf[4];
#pragma unroll
    for (int i = 0; i < 4; ++i) {
      om[i] = lds_rd128(aux + (c & 7) * 256 + (4 * i + g) * 16);
      qf[i] = lds_rd128(aux + 2048 + (4 * i + g) * 16);
    }
    f32x4 qp = {0.f, 0.f, 0.f, 0.f};
    float qs = 0.f, qs2 = 0.f;
#pragma unroll
    for (int i = 0; i < 4; ++i) {
#pragma unroll
      for (int j = 0; j < 4; ++j) {
        qf[i][j] = scale_pk(qf[i][j], SCALE);
        if (j & 1) qs2 = dot2(qf[i][j], qf[i][j], qs2); else qs = dot2(qf[i][j], qf[i][j], qs);
        omX[i][j] = c < 8 ? om[i][j] : 0u;
        omY[i][j] = c < 8 ? 0u : om[i][j];
      }
      qp = mfma32(om[i], qf[i], qp);
    }
    f32x4 qq = {0.f, 0.f, 0.f, 0.f};
    qq = mfma4f(1.0f, qs + qs2, qq);
    const u32x2 pq = phi4(qp, qq);
    phq[0] = bf_lo(pq[0]); phq[1] = bf_hi(pq[0]); phq[2] = bf_lo(pq[1]); phq[3] = bf_hi(pq[1]);
  }
  const float oneX = c < 8 ? 1.0f : 0.0f, oneY = c < 8 ? 0.0f : 1.0f;
  asm volatile("" : "+v"(phq));
  wait_lgkm0();

#pragma unroll 1
  for (int p = 0; p < TPW / 2; ++p) {
    const u32 so = (u32)((2 * p) & (NSLOT - 1)) * TILE_B;
    const bool nk = p < (TPW - NSLOT) / 2;
    const char* nsrc = nk ? kbase + (size_t)(2 * p + NSLOT) * TSTEP : vbase + (size_t)(2 * p + NSLOT - TPW) * TSTEP;
    u32 no[4];
#pragma unroll
    for (int i = 0; i < 4; ++i) no[i] = nk ? koff[i] : voffs[i];
    u32x4 kx[4], ky[4];
    wait_vm<4 * (NSLOT - 1)>();
#pragma unroll
    for (int i = 0; i < 4; ++i) kx[i] = lds_rd128(kra[i] + so);
    wait_lgkm0();
    fill4k(nsrc, no, lds0 + so);
    wait_vm<4 * (NSLOT - 1)>();
#pragma unroll
    for (int i = 0; i < 4; ++i) ky[i] = lds_rd128(kra[i] + so + TILE_B);
    wait_lgkm0();
    fill4k(nsrc + TSTEP, no, lds0 + so + TILE_B);
    PIN4(kx); PIN4(ky);
    if (p == 4 && wave == 0 && split == 0) {
      echoed = *(const LAS u32*)(size_t)(ctl + lane * 16);
      if (lane < 3) st8(hrec + lane * REC_B + OFF_CHAL, tagv, echoed);
    }

    f32x4 pr = {0.f, 0.f, 0.f, 0.f};
    float sx = 0.f, sy = 0.f, sx2 = 0.f, sy2 = 0.f;
#pragma unroll
    for (int i = 0; i < 4; ++i) {
#pragma unroll
      for (int j = 0; j < 4; ++j) { kx[i][j] = scale_pk(kx[i][j], SCALE); if (j & 1) sx2 = dot2(kx[i][j], kx[i][j], sx2); else sx = dot2(kx[i][j], kx[i][j], sx); }
      pr = mfma32(kx[i], omX[i], pr);
    }
#pragma unroll
    for (int i = 0; i < 4; ++i) {
#pragma unroll
      for (int j = 0; j < 4; ++j) { ky[i][j] = scale_pk(ky[i][j], SCALE); if (j & 1) sy2 = dot2(ky[i][j], ky[i][j], sy2); else sy = dot2(ky[i][j], ky[i][j], sy); }
      pr = mfma32(ky[i], omY[i], pr);
    }
    f32x4 sq = {0.f, 0.f, 0.f, 0.f};
    sq = mfma4f(sx + sx2, oneX, sq);
    sq = mfma4f(sy + sy2, oneY, sq);
    const u32x2 ph = phi4(pr, sq);
    *(LAS u32x2*)(size_t)(aux + p * 512 + lane * 8) = ph;
  }

  STAMPV(3);
  f32x4 acc[8], zacc = {0.f, 0.f, 0.f, 0.f};
#pragma unroll
  for (int b = 0; b < 8; ++b) acc[b] = f32x4{0.f, 0.f, 0.f, 0.f};
  const u32x4 ones = {0x3f803f80u, 0x3f803f80u, 0x3f803f80u, 0x3f803f80u};

  auto vstep = [&](int p, u32 so, auto waitx, auto fillx, auto waity, auto filly) {
    const u32x2 ph = *(const LAS u32x2*)(size_t)(aux + p * 512 + lane * 8);
    u32x4 a;
    a[0] = c < 8 ? ph[0] : 0u; a[1] = c < 8 ? ph[1] : 0u; a[2] = c < 8 ? 0u : ph[0]; a[3] = c < 8 ? 0u : ph[1];
    u32x2 vx[8], vy[8];
    waitx();
#pragma unroll
    for (int b = 0; b < 8; ++b) vx[b] = lds_tr(vra[b] + so);
    fillx();
    waity();
#pragma unroll
    for (int b = 0; b < 8; ++b) vy[b] = lds_tr(vra[b] + so + TILE_B);
    filly();
    PIN8(vx); PIN8(vy);
    zacc = mfma32(a, ones, zacc);
#pragma unroll
    for (int b = 0; b < 8; ++b) {
      const u32x4 bb = {vx[b][0], vx[b][1], vy[b][0], vy[b][1]};
      acc[b] = mfma32(a, bb, acc[b]);
    }
  };
#pragma unroll 1
  for (int p = 0; p < (TPW - NSLOT) / 2; ++p) {
    const u32 so = (u32)((2 * p) & (NSLOT - 1)) * TILE_B;
    const char* nsrc = vbase + (size_t)(2 * p + NSLOT) * TSTEP;
    vstep(p, so, [&] { wait_vm<4 * (NSLOT - 1)>(); }, [&] { wait_lgkm0(); fill4k(nsrc, voffs, lds0 + so); },
          [&] { wait_vm<4 * (NSLOT - 1)>(); }, [&] { wait_lgkm0(); fill4k(nsrc + TSTEP, voffs, lds0 + so + TILE_B); });
  }
  if (wave == 0 && split != 0) dma1k_sc1(myrec + OFF_CHAL, 0u, ctl);
  auto nop = [] {};
  vstep(4, 0 * TILE_B, [&] { wait_vm<28>(); }, nop, [&] { wait_vm<24>(); }, nop);
  vstep(5, 2 * TILE_B, [&] { wait_vm<20>(); }, nop, [&] { wait_vm<16>(); }, nop);
  vstep(6, 4 * TILE_B, [&] { wait_vm<12>(); }, nop, [&] { wait_vm<8>(); }, nop);
  vstep(7, 6 * TILE_B, [&] { wait_vm<4>(); }, nop, [&] { wait_vm<0>(); }, nop);

  STAMPV(4);
  float t[8];
#pragma unroll
  for (int b = 0; b < 8; ++b)
    t[b] = fmaf(phq[3], acc[b][3], fmaf(phq[2], acc[b][2], fmaf(phq[1], acc[b][1], phq[0] * acc[b][0])));
  float dn = fmaf(phq[3], zacc[3], fmaf(phq[2], zacc[2], fmaf(phq[1], zacc[1], phq[0] * zacc[0])));
  float u[4];
#pragma unroll
  for (int b = 0; b < 4; ++b) {
    auto s = __builtin_amdgcn_permlane32_swap(__float_as_uint(t[b]), __float_as_uint(t[b + 4]), false, false);
    u[b] = __uint_as_float(s[0]) + __uint_as_float(s[1]);
  }
  float w2[2];
#pragma unroll
  for (int b = 0; b < 2; ++b) {
    auto s = __builtin_amdgcn_permlane16_swap(__float_as_uint(u[b]), __float_as_uint(u[b + 2]), false, false);
    w2[b] = __uint_as_float(s[0]) + __uint_as_float(s[1]);
  }
  {
    auto s = __builtin_amdgcn_permlane32_swap(__float_as_uint(dn), __float_as_uint(dn), false, false);
    dn = __uint_as_float(s[0]) + __uint_as_float(s[1]);
    auto s2 = __builtin_amdgcn_permlane16_swap(__float_as_uint(dn), __float_as_uint(dn), false, false);
    dn = __uint_as_float(s2[0]) + __uint_as_float(s2[1]);
  }
  LAS float* red = (LAS float*)(size_t)lds0;
  red[32 * g + c] = w2[0];
  red[32 * g + 16 + c] = w2[1];
  if (lane == 0) red[128] = dn;
  __syncthreads();
  if (wave == 0) {
    const u32 base = (u32)(size_t)(LAS char*)smem;
    float n0 = 0.f, n1 = 0.f, dsum = 0.f;
#pragma unroll
    for (int w = 0; w < NWAVES; ++w) {
      const f32x2 x = *(const LAS f32x2*)(size_t)(base + w * WAVE_LDS + lane * 8);
      n0 += x[0]; n1 += x[1]; dsum += *(const LAS float*)(size_t)(base + w * WAVE_LDS + 512);
    }
    const u64 t0 = __builtin_amdgcn_s_memrealtime();
    if (split != 0) {
      const u32x2 cw = *(const LAS u32x2*)(size_t)ctl;
      u32 nn = __builtin_amdgcn_readfirstlane(cw[0]), ee = __builtin_amdgcn_readfirstlane(cw[1]);
      while (ee != tagv) {
        const u64 x = ld8(myrec + OFF_CHAL);
        nn = __builtin_amdgcn_readfirstlane((u32)x); ee = __builtin_amdgcn_readfirstlane((u32)(x >> 32));
        if (__builtin_amdgcn_s_memrealtime() - t0 > 20000000ull) break;
        __builtin_amdgcn_s_sleep(2);
      }
      st8(myrec + lane * 16, __float_as_uint(n0), nn);
      st8(myrec + lane * 16 + 8, __float_as_uint(n1), nn);
      if (lane == 0) st8(myrec + OFF_DEN, __float_as_uint(dsum), nn);
    } else {
      u64 x[9];
      for (;;) {
#pragma unroll
        for (int j = 0; j < 3; ++j) {
          x[3 * j] = ld8(hrec + j * REC_B + lane * 16);
          x[3 * j + 1] = ld8(hrec + j * REC_B + lane * 16 + 8);
          x[3 * j + 2] = ld8(hrec + j * REC_B + OFF_DEN);
        }
        const u32 hv = ld4(hrec + j3 * REC_B + OFF_HELLO);
        bool ok = true;
#pragma unroll
        for (int k = 0; k < 9; ++k) ok = ok && ((u32)(x[k] >> 32) == tagv);
        if (__all(ok)) break;
        if (lane < 3 && hv != echoed) { echoed = hv; st8(hrec + lane * REC_B + OFF_CHAL, tagv, hv); }
        if (__builtin_amdgcn_s_memrealtime() - t0 > 20000000ull) break;
      }
#pragma unroll
      for (int j = 0; j < 3; ++j) {
        n0 += __uint_as_float((u32)x[3 * j]); n1 += __uint_as_float((u32)x[3 * j + 1]); dsum += __uint_as_float((u32)x[3 * j + 2]);
      }
      const float den = rbf(rbf(dsum) + 1e-6f);
      out[bh * (D / 2) + lane] = pk_bf16(rbf(n0) / den, rbf(n1) / den);
    }
  }
  STAMPV(5);
  if (lane == 0) for (int k = 0; k < NSTAMP; ++k) __hip_atomic_store((GAS u64*)(ws + STAMP_OFF) + (blockIdx.x * 4 + wave) * NSTAMP + k, stv[k], __ATOMIC_RELAXED, __HIP_MEMORY_SCOPE_AGENT);
  if (wave == 0) {
    while (__builtin_amdgcn_s_memrealtime() - stv[0] < 3200ull) __builtin_amdgcn_s_sleep(1);
    const u64 now = __builtin_amdgcn_s_memrealtime();
    u32 first = 0xffffffffu, mx = 0, mn = 0xffffffffu, sum = 0, cnt = 0;
    const u64 b0 = now - 4500ull;
    for (int w = lane; w < 1024; w += 64) {
      const u64 s0 = ld8(ws + STAMP_OFF + (w * NSTAMP + 0) * 8) - b0;
      const u64 sk = ld8(ws + STAMP_OFF + (w * NSTAMP + DIAGK) * 8) - b0;
      if (s0 <= 4500ull) first = min(first, (u32)s0);
      if (sk <= 4500ull) { mx = max(mx, (u32)sk); mn = min(mn, (u32)sk); sum += (u32)sk; ++cnt; }
    }
    for (int o = 1; o < 64; o <<= 1) { first = min(first, (u32)__shfl_xor((int)first, o)); mx = max(mx, (u32)__shfl_xor((int)mx, o)); mn = min(mn, (u32)__shfl_xor((int)mn, o)); sum += (u32)__shfl_xor((int)sum, o); cnt += (u32)__shfl_xor((int)cnt, o); }
    const u32 x = (DIAGMAX == 2 ? sum / max(cnt, 1u) : DIAGMAX ? mx : mn) - first;
    const u64 tend = b0 + first + 6000ull + (u64)(x * DIAGAMP);
    while (__builtin_amdgcn_s_memrealtime() < tend) __builtin_amdgcn_s_sleep(1);
  }
}

extern "C" void kernel_launch(void* const* d_in, const int* in_sizes, int n_in,
                              void* d_out, int out_size, void* d_ws, size_t ws_size,
                              hipStream_t stream) {
  rfa_main<<<NBH * NSPLIT, NWAVES * 64, 0, stream>>>((const char*)d_in[0], (const char*)d_in[1], (const char*)d_in[2],
                                                      (const char*)d_in[3], (char*)d_ws, (u32*)d_out);
}
```

```cpp
#define DIAGK 4
#define DIAGMAX 1
#define DIAGAMP 1
#define XSWZ 0
#include <hip/hip_runtime.h>
#include <hip/hip_bf16.h>
#include <stdint.h>

#define VTOUCH 0
typedef unsigned int u32;
typedef unsigned long long u64;
using u32x2 = __attribute__((ext_vector_type(2))) unsigned;
using u32x4 = __attribute__((ext_vector_type(4))) unsigned;
using f32x2 = __attribute__((ext_vector_type(2))) float;
using f32x4 = __attribute__((ext_vector_type(4))) float;
using bf16x2 = __attribute__((ext_vector_type(2))) __bf16;
using bf16x8 = __attribute__((ext_vector_type(8))) __bf16;
using s16x4 = __attribute__((ext_vector_type(4))) short;
#define LAS __attribute__((address_space(3)))
#define GAS __attribute__((address_space(1)))

constexpr int NBH = 64, SK = 4096, D = 128, M = 8;
constexpr int NSPLIT = 4, NWAVES = 4;
constexpr int TILE_B = 4096;
constexpr int NSLOT = 8;
constexpr int TPW = SK / NSPLIT / 16 / NWAVES;
constexpr int WAVE_LDS = NSLOT * TILE_B + 4096;
constexpr int CTL_LDS = NWAVES * WAVE_LDS;
constexpr int LDS_BYTES = CTL_LDS + 4096;
constexpr int REC_B = 2048, HEAD_B = 4 * REC_B;
constexpr int OFF_DEN = 1024, OFF_HELLO = 1280, OFF_CHAL = 1536;

__device__ __forceinline__ float bf_lo(u32 x) { return __uint_as_float(x << 16); }
__device__ __forceinline__ float bf_hi(u32 x) { return __uint_as_float(x & 0xffff0000u); }
__device__ __forceinline__ u32 pk_bf16(float a, float b) {
  f32x2 f = {a, b};
  return __builtin_bit_cast(u32, __builtin_convertvector(f, bf16x2));
}
__device__ __forceinline__ u32 scale_pk(u32 x, float s) {
  f32x2 f = {bf_lo(x), bf_hi(x)};
  f = f * s;
  return __builtin_bit_cast(u32, __builtin_convertvector(f, bf16x2));
}
__device__ __forceinline__ float dot2(u32 a, u32 b, float c) {
  return __builtin_amdgcn_fdot2_f32_bf16(__builtin_bit_cast(bf16x2, a), __builtin_bit_cast(bf16x2, b), c, false);
}
__device__ __forceinline__ f32x4 mfma32(u32x4 a, u32x4 b, f32x4 c) {
  return __builtin_amdgcn_mfma_f32_16x16x32_bf16(__builtin_bit_cast(bf16x8, a), __builtin_bit_cast(bf16x8, b), c, 0, 0, 0);
}
__device__ __forceinline__ f32x4 mfma4f(float a, float b, f32x4 c) { return __builtin_amdgcn_mfma_f32_16x16x4f32(a, b, c, 0, 0, 0); }

__device__ __forceinline__ void fill4k(const char* sbase, const u32 (&voff)[4], u32 lds) {
  u32 keep;
  asm volatile("s_mov_b32 %0, m0\n\ts_mov_b32 m0, %6\n\ts_nop 0\n\t"
               "global_load_lds_dwordx4 %1, %5 offset:0 nt\n\t"
               "global_load_lds_dwordx4 %2, %5 offset:1024 nt\n\t"
               "global_load_lds_dwordx4 %3, %5 offset:2048 nt\n\t"
               "global_load_lds_dwordx4 %4, %5 offset:3072 nt\n\t"
               "s_mov_b32 m0, %0"
               : "=&s"(keep) : "v"(voff[0]), "v"(voff[1]), "v"(voff[2]), "v"(voff[3]), "s"(sbase), "s"(lds) : "memory");
}
__device__ __forceinline__ void dma1k(const char* sbase, u32 voff, u32 lds) {
  u32 keep;
  asm volatile("s_mov_b32 %0, m0\n\ts_mov_b32 m0, %3\n\ts_nop 0\n\t"
               "global_load_lds_dwordx4 %1, %2\n\t"
               "s_mov_b32 m0, %0"
               : "=&s"(keep) : "v"(voff), "s"(sbase), "s"(lds) : "memory");
}
__device__ __forceinline__ void dma1k_sc1(const char* sbase, u32 voff, u32 lds) {
  u32 keep;
  asm volatile("s_mov_b32 %0, m0\n\ts_mov_b32 m0, %3\n\ts_nop 0\n\t"
               "global_load_lds_dwordx4 %1, %2 sc1\n\t"
               "s_mov_b32 m0, %0"
               : "=&s"(keep) : "v"(voff), "s"(sbase), "s"(lds) : "memory");
}
__device__ __forceinline__ void st8(char* p, u32 lo, u32 hi) {
  __hip_atomic_store((GAS u64*)p, ((u64)hi << 32) | lo, __ATOMIC_RELAXED, __HIP_MEMORY_SCOPE_AGENT);
}
__device__ __forceinline__ void st4(char* p, u32 v) { __hip_atomic_store((GAS u32*)p, v, __ATOMIC_RELAXED, __HIP_MEMORY_SCOPE_AGENT); }
__device__ __forceinline__ u64 ld8(const char* p) { return __hip_atomic_load((GAS u64*)p, __ATOMIC_RELAXED, __HIP_MEMORY_SCOPE_AGENT); }
__device__ __forceinline__ u32 ld4(const char* p) { return __hip_atomic_load((GAS u32*)p, __ATOMIC_RELAXED, __HIP_MEMORY_SCOPE_AGENT); }
__device__ __forceinline__ float rbf(float x) { return bf_lo(pk_bf16(x, x)); }
template <int N> __device__ __forceinline__ void wait_vm() { asm volatile("s_waitcnt vmcnt(%0)" :: "i"(N) : "memory"); }
__device__ __forceinline__ void wait_lgkm0() { asm volatile("s_waitcnt lgkmcnt(0)" ::: "memory"); }
#define PIN4(x) asm volatile("" : "+v"(x[0]), "+v"(x[1]), "+v"(x[2]), "+v"(x[3]) :: "memory")
#define PIN8(x) asm volatile("" : "+v"(x[0]), "+v"(x[1]), "+v"(x[2]), "+v"(x[3]), "+v"(x[4]), "+v"(x[5]), "+v"(x[6]), "+v"(x[7]) :: "memory")
__device__ __forceinline__ u32x4 lds_rd128(u32 addr) { return *(const LAS u32x4*)(size_t)addr; }
__device__ __forceinline__ u32x2 lds_tr(u32 addr) {
  return __builtin_bit_cast(u32x2, __builtin_amdgcn_ds_read_tr16_b64_v4i16((LAS s16x4*)(size_t)addr));
}

__device__ __forceinline__ u32x2 phi4(f32x4 pr, f32x4 sq) {
  const u32 p01 = pk_bf16(pr[0], pr[1]), p23 = pk_bf16(pr[2], pr[3]);
  const u32 s01 = pk_bf16(sq[0], sq[1]), s23 = pk_bf16(sq[2], sq[3]);
  const u32 x01 = pk_bf16(fmaf(-0.5f, bf_lo(s01), bf_lo(p01)), fmaf(-0.5f, bf_hi(s01), bf_hi(p01)));
  const u32 x23 = pk_bf16(fmaf(-0.5f, bf_lo(s23), bf_lo(p23)), fmaf(-0.5f, bf_hi(s23), bf_hi(p23)));
  const float L2E = 1.4426950408889634f;
  const u32 e01 = pk_bf16(__builtin_amdgcn_exp2f(bf_lo(x01) * L2E), __builtin_amdgcn_exp2f(bf_hi(x01) * L2E));
  const u32 e23 = pk_bf16(__builtin_amdgcn_exp2f(bf_lo(x23) * L2E), __builtin_amdgcn_exp2f(bf_hi(x23) * L2E));
  const float R = 0.35355339059327373f;
  const u32 y01 = pk_bf16(bf_lo(e01) * R, bf_hi(e01) * R), y23 = pk_bf16(bf_lo(e23) * R, bf_hi(e23) * R);
  u32x2 o;
  o[0] = pk_bf16(bf_lo(y01) + 1e-6f, bf_hi(y01) + 1e-6f);
  o[1] = pk_bf16(bf_lo(y23) + 1e-6f, bf_hi(y23) + 1e-6f);
  return o;
}

#define STAMPS 1
#define NSTAMP 8
#define STAMP_OFF (1 << 20)
#define STAMP(k) do { const u64 t_ = __builtin_amdgcn_s_memrealtime(); if (lane == 0) ((u64*)(ws + STAMP_OFF))[(blockIdx.x * 4 + wave) * NSTAMP + (k)] = t_; } while (0)
#define STAMPV(k) stv[k] = __builtin_amdgcn_s_memrealtime()
__global__ void __launch_bounds__(NWAVES * 64, 1)
rfa_main(const char* __restrict__ qg, const char* __restrict__ kg, const char* __restrict__ vg, const char* __restrict__ og,
         char* __restrict__ ws, u32* __restrict__ out) {
  __shared__ __attribute__((aligned(16))) char smem[LDS_BYTES];
  const int lane = threadIdx.x & 63, wave = __builtin_amdgcn_readfirstlane(threadIdx.x >> 6);
  const int g = lane >> 4, c = lane & 15;
  u64 stv[NSTAMP];
  STAMPV(0);
  const int chunk = blockIdx.x;
  const int bh = chunk >> 2, split = chunk & 3;
  const float SCALE = 0.29730177875068026f;
  const u32 lds0 = (u32)(size_t)(LAS char*)smem + wave * WAVE_LDS;
  const u32 aux = lds0 + NSLOT * TILE_B;

  u32 koff[4], voffs[4];
#pragma unroll
  for (int p = 0; p < 4; ++p) {
    const u32 row = 4 * p + g;
    koff[p] = g * 256 + ((c ^ row) << 4);
    voffs[p] = g * 256 + ((c ^ ((row & 7) << 1)) << 4);
  }
  const size_t wg_off = ((size_t)bh * SK + (size_t)split * (SK / NSPLIT)) * (D * 2) + (size_t)wave * TILE_B;
  const char* kbase = kg + wg_off;
  const char* vbase = vg + wg_off;
  constexpr size_t TSTEP = (size_t)NWAVES * TILE_B;

  const u32 ctl = (u32)(size_t)(LAS char*)smem + CTL_LDS;
  char* hrec = ws + (size_t)bh * HEAD_B;
  char* myrec = hrec + (split - 1) * REC_B;
  const u32 j3 = lane < 2 ? lane : 2;
  asm volatile("s_memrealtime %0\n\ts_waitcnt lgkmcnt(0)" : "=s"(stv[6]) : "s"(kbase), "s"(hrec) : "memory");
  if (wave == 0) {
    if (split == 0) {
      dma1k_sc1(hrec, lane * 16, ctl);
      dma1k_sc1(hrec + REC_B, lane * 16, ctl + 1024);
      dma1k_sc1(hrec + 2 * REC_B, lane * 16, ctl + 2048);
      dma1k_sc1(hrec + OFF_DEN, j3 * REC_B, ctl + 3072);
    } else {
      dma1k_sc1(myrec + OFF_HELLO, (lane & 1) * (OFF_CHAL - OFF_HELLO), ctl);
    }
  }
  dma1k(og, lane * 16, aux);
  dma1k(og + 1024, lane * 16, aux + 1024);
  dma1k(qg + bh * (D * 2), c * 16, aux + 2048);
#pragma unroll
  for (int f = 0; f < NSLOT; ++f) { fill4k(kbase + f * TSTEP, koff, lds0 + f * TILE_B);
    if (f == 0) asm volatile("s_memrealtime %0\n\ts_waitcnt lgkmcnt(0)" : "=s"(stv[7]) :: "memory"); }

  STAMPV(1);
  u32 kra[4], vra[8];
#pragma unroll
  for (int i = 0; i < 4; ++i) kra[i] = lds0 + c * 256 + (((4 * i + g) ^ c) << 4);
  {
    const u32 row = 4 * g + (c >> 2);
#pragma unroll
    for (int b = 0; b < 8; ++b) vra[b] = lds0 + row * 256 + ((b ^ (row & 7)) << 5) + (c & 3) * 8;
  }

  wait_vm<4 * NSLOT + VTOUCH>();
  asm volatile("s_memrealtime %0\n\ts_waitcnt lgkmcnt(0)" : "=s"(stv[2]) :: "memory");
  u32 tagv = 0;
  if (wave == 0) {
    if (split == 0) {
      const u32x4 a0 = lds_rd128(ctl + lane * 16), a1 = lds_rd128(ctl + 1024 + lane * 16), a2 = lds_rd128(ctl + 2048 + lane * 16), a3 = lds_rd128(ctl + 3072 + lane * 16);
      u32 cand = __builtin_amdgcn_readfirstlane(a0[1]) + 1u;
      while (__any(a0[1] == cand || a0[3] == cand || a1[1] == cand || a1[3] == cand || a2[1] == cand || a2[3] == cand || a3[1] == cand)) ++cand;
      tagv = cand;
      wait_lgkm0();
      dma1k_sc1(hrec + OFF_HELLO, j3 * REC_B, ctl);
    } else {
      const u32 h0 = *(const LAS u32*)(size_t)ctl, e0 = *(const LAS u32*)(size_t)(ctl + 16 + 4);
      u32 pv = __builtin_amdgcn_readfirstlane(h0) + 1u;
      if (pv == (u32)__builtin_amdgcn_readfirstlane(e0)) ++pv;
      tagv = pv;
      if (lane == 0) st4(myrec + OFF_HELLO, pv);
    }
  }
  u32 echoed = 0;
  u32x4 omX[4], omY[4];
  f32x4 phq;
  {
    u32x4 om[4], qf[4];
#pragma unroll
    for (int i = 0; i < 4; ++i) {
      om[i] = lds_rd128(aux + (c & 7) * 256 + (4 * i + g) * 16);
      qf[i] = lds_rd128(aux + 2048 + (4 * i + g) * 16);
    }
    f32x4 qp = {0.f, 0.f, 0.f, 0.f};
    float qs = 0.f, qs2 = 0.f;
#pragma unroll
    for (int i = 0; i < 4; ++i) {
#pragma unroll
      for (int j = 0; j < 4; ++j) {
        qf[i][j] = scale_pk(qf[i][j], SCALE);
        if (j & 1) qs2 = dot2(qf[i][j], qf[i][j], qs2); else qs = dot2(qf[i][j], qf[i][j], qs);
        omX[i][j] = c < 8 ? om[i][j] : 0u;
        omY[i][j] = c < 8 ? 0u : om[i][j];
      }
      qp = mfma32(om[i], qf[i], qp);
    }
    f32x4 qq = {0.f, 0.f, 0.f, 0.f};
    qq = mfma4f(1.0f, qs + qs2, qq);
    const u32x2 pq = phi4(qp, qq);
    phq[0] = bf_lo(pq[0]); phq[1] = bf_hi(pq[0]); phq[2] = bf_lo(pq[1]); phq[3] = bf_hi(pq[1]);
  }
  const float oneX = c < 8 ? 1.0f : 0.0f, oneY = c < 8 ? 0.0f : 1.0f;
  asm volatile("" : "+v"(phq));
  wait_lgkm0();

#pragma unroll 1
  for (int p = 0; p < TPW / 2; ++p) {
    const u32 so = (u32)((2 * p) & (NSLOT - 1)) * TILE_B;
    const bool nk = p < (TPW - NSLOT) / 2;
    const char* nsrc = nk ? kbase + (size_t)(2 * p + NSLOT) * TSTEP : vbase + (size_t)(2 * p + NSLOT - TPW) * TSTEP;
    u32 no[4];
#pragma unroll
    for (int i = 0; i < 4; ++i) no[i] = nk ? koff[i] : voffs[i];
    u32x4 kx[4], ky[4];
    wait_vm<4 * (NSLOT - 1)>();
#pragma unroll
    for (int i = 0; i < 4; ++i) kx[i] = lds_rd128(kra[i] + so);
    wait_lgkm0();
    fill4k(nsrc, no, lds0 + so);
    wait_vm<4 * (NSLOT - 1)>();
#pragma unroll
    for (int i = 0; i < 4; ++i) ky[i] = lds_rd128(kra[i] + so + TILE_B);
    wait_lgkm0();
    fill4k(nsrc + TSTEP, no, lds0 + so + TILE_B);
    PIN4(kx); PIN4(ky);
    if (p == 4 && wave == 0 && split == 0) {
      echoed = *(const LAS u32*)(size_t)(ctl + lane * 16);
      if (lane < 3) st8(hrec + lane * REC_B + OFF_CHAL, tagv, echoed);
    }

    f32x4 pr = {0.f, 0.f, 0.f, 0.f};
    float sx = 0.f, sy = 0.f, sx2 = 0.f, sy2 = 0.f;
#pragma unroll
    for (int i = 0; i < 4; ++i) {
#pragma unroll
      for (int j = 0; j < 4; ++j) { kx[i][j] = scale_pk(kx[i][j], SCALE); if (j & 1) sx2 = dot2(kx[i][j], kx[i][j], sx2); else sx = dot2(kx[i][j], kx[i][j], sx); }
      pr = mfma32(kx[i], omX[i], pr);
    }
#pragma unroll
    for (int i = 0; i < 4; ++i) {
#pragma unroll
      for (int j = 0; j < 4; ++j) { ky[i][j] = scale_pk(ky[i][j], SCALE); if (j & 1) sy2 = dot2(ky[i][j], ky[i][j], sy2); else sy = dot2(ky[i][j], ky[i][j], sy); }
      pr = mfma32(ky[i], omY[i], pr);
    }
    f32x4 sq = {0.f, 0.f, 0.f, 0.f};
    sq = mfma4f(sx + sx2, oneX, sq);
    sq = mfma4f(sy + sy2, oneY, sq);
    const u32x2 ph = phi4(pr, sq);
    *(LAS u32x2*)(size_t)(aux + p * 512 + lane * 8) = ph;
  }

  STAMPV(3);
  f32x4 acc[8], zacc = {0.f, 0.f, 0.f, 0.f};
#pragma unroll
  for (int b = 0; b < 8; ++b) acc[b] = f32x4{0.f, 0.f, 0.f, 0.f};
  const u32x4 ones = {0x3f803f80u, 0x3f803f80u, 0x3f803f80u, 0x3f803f80u};

  auto vstep = [&](int p, u32 so, auto waitx, auto fillx, auto waity, auto filly) {
    const u32x2 ph = *(const LAS u32x2*)(size_t)(aux + p * 512 + lane * 8);
    u32x4 a;
    a[0] = c < 8 ? ph[0] : 0u; a[1] = c < 8 ? ph[1] : 0u; a[2] = c < 8 ? 0u : ph[0]; a[3] = c < 8 ? 0u : ph[1];
    u32x2 vx[8], vy[8];
    waitx();
#pragma unroll
    for (int b = 0; b < 8; ++b) vx[b] = lds_tr(vra[b] + so);
    fillx();
    waity();
#pragma unroll
    for (int b = 0; b < 8; ++b) vy[b] = lds_tr(vra[b] + so + TILE_B);
    filly();
    PIN8(vx); PIN8(vy);
    zacc = mfma32(a, ones, zacc);
#pragma unroll
    for (int b = 0; b < 8; ++b) {
      const u32x4 bb = {vx[b][0], vx[b][1], vy[b][0], vy[b][1]};
      acc[b] = mfma32(a, bb, acc[b]);
    }
  };
#pragma unroll 1
  for (int p = 0; p < (TPW - NSLOT) / 2; ++p) {
    const u32 so = (u32)((2 * p) & (NSLOT - 1)) * TILE_B;
    const char* nsrc = vbase + (size_t)(2 * p + NSLOT) * TSTEP;
    vstep(p, so, [&] { wait_vm<4 * (NSLOT - 1)>(); }, [&] { wait_lgkm0(); fill4k(nsrc, voffs, lds0 + so); },
          [&] { wait_vm<4 * (NSLOT - 1)>(); }, [&] { wait_lgkm0(); fill4k(nsrc + TSTEP, voffs, lds0 + so + TILE_B); });
  }
  if (wave == 0 && split != 0) dma1k_sc1(myrec + OFF_CHAL, 0u, ctl);
  auto nop = [] {};
  vstep(4, 0 * TILE_B, [&] { wait_vm<28>(); }, nop, [&] { wait_vm<24>(); }, nop);
  vstep(5, 2 * TILE_B, [&] { wait_vm<20>(); }, nop, [&] { wait_vm<16>(); }, nop);
  vstep(6, 4 * TILE_B, [&] { wait_vm<12>(); }, nop, [&] { wait_vm<8>(); }, nop);
  vstep(7, 6 * TILE_B, [&] { wait_vm<4>(); }, nop, [&] { wait_vm<0>(); }, nop);

  STAMPV(4);
  float t[8];
#pragma unroll
  for (int b = 0; b < 8; ++b)
    t[b] = fmaf(phq[3], acc[b][3], fmaf(phq[2], acc[b][2], fmaf(phq[1], acc[b][1], phq[0] * acc[b][0])));
  float dn = fmaf(phq[3], zacc[3], fmaf(phq[2], zacc[2], fmaf(phq[1], zacc[1], phq[0] * zacc[0])));
  float u[4];
#pragma unroll
  for (int b = 0; b < 4; ++b) {
    auto s = __builtin_amdgcn_permlane32_swap(__float_as_uint(t[b]), __float_as_uint(t[b + 4]), false, false);
    u[b] = __uint_as_float(s[0]) + __uint_as_float(s[1]);
  }
  float w2[2];
#pragma unroll
  for (int b = 0; b < 2; ++b) {
    auto s = __builtin_amdgcn_permlane16_swap(__float_as_uint(u[b]), __float_as_uint(u[b + 2]), false, false);
    w2[b] = __uint_as_float(s[0]) + __uint_as_float(s[1]);
  }
  {
    auto s = __builtin_amdgcn_permlane32_swap(__float_as_uint(dn), __float_as_uint(dn), false, false);
    dn = __uint_as_float(s[0]) + __uint_as_float(s[1]);
    auto s2 = __builtin_amdgcn_permlane16_swap(__float_as_uint(dn), __float_as_uint(dn), false, false);
    dn = __uint_as_float(s2[0]) + __uint_as_float(s2[1]);
  }
  LAS float* red = (LAS float*)(size_t)lds0;
  red[32 * g + c] = w2[0];
  red[32 * g + 16 + c] = w2[1];
  if (lane == 0) red[128] = dn;
  __syncthreads();
  if (wave == 0) {
    const u32 base = (u32)(size_t)(LAS char*)smem;
    float n0 = 0.f, n1 = 0.f, dsum = 0.f;
#pragma unroll
    for (int w = 0; w < NWAVES; ++w) {
      const f32x2 x = *(const LAS f32x2*)(size_t)(base + w * WAVE_LDS + lane * 8);
      n0 += x[0]; n1 += x[1]; dsum += *(const LAS float*)(size_t)(base + w * WAVE_LDS + 512);
    }
    const u64 t0 = __builtin_amdgcn_s_memrealtime();
    if (split != 0) {
      const u32x2 cw = *(const LAS u32x2*)(size_t)ctl;
      u32 nn = __builtin_amdgcn_readfirstlane(cw[0]), ee = __builtin_amdgcn_readfirstlane(cw[1]);
      while (ee != tagv) {
        const u64 x = ld8(myrec + OFF_CHAL);
        nn = __builtin_amdgcn_readfirstlane((u32)x); ee = __builtin_amdgcn_readfirstlane((u32)(x >> 32));
        if (__builtin_amdgcn_s_memrealtime() - t0 > 20000000ull) break;
        __builtin_amdgcn_s_sleep(2);
      }
      st8(myrec + lane * 16, __float_as_uint(n0), nn);
      st8(myrec + lane * 16 + 8, __float_as_uint(n1), nn);
      if (lane == 0) st8(myrec + OFF_DEN, __float_as_uint(dsum), nn);
    } else {
      u64 x[9];
      for (;;) {
#pragma unroll
        for (int j = 0; j < 3; ++j) {
          x[3 * j] = ld8(hrec + j * REC_B + lane * 16);
          x[3 * j + 1] = ld8(hrec + j * REC_B + lane * 16 + 8);
          x[3 * j + 2] = ld8(hrec + j * REC_B + OFF_DEN);
        }
        const u32 hv = ld4(hrec + j3 * REC_B + OFF_HELLO);
        bool ok = true;
#pragma unroll
        for (int k = 0; k < 9; ++k) ok = ok && ((u32)(x[k] >> 32) == tagv);
        if (__all(ok)) break;
        if (lane < 3 && hv != echoed) { echoed = hv; st8(hrec + lane * REC_B + OFF_CHAL, tagv, hv); }
        if (__builtin_amdgcn_s_memrealtime() - t0 > 20000000ull) break;
      }
#pragma unroll
      for (int j = 0; j < 3; ++j) {
        n0 += __uint_as_float((u32)x[3 * j]); n1 += __uint_as_float((u32)x[3 * j + 1]); dsum += __uint_as_float((u32)x[3 * j + 2]);
      }
      const float den = rbf(rbf(dsum) + 1e-6f);
      out[bh * (D / 2) + lane] = pk_bf16(rbf(n0) / den, rbf(n1) / den);
    }
  }
  STAMPV(5);
  if (lane == 0) for (int k = 0; k < NSTAMP; ++k) __hip_atomic_store((GAS u64*)(ws + STAMP_OFF) + (blockIdx.x * 4 + wave) * NSTAMP + k, stv[k], __ATOMIC_RELAXED, __HIP_MEMORY_SCOPE_AGENT);
  if (wave == 0) {
    while (__builtin_amdgcn_s_memrealtime() - stv[0] < 3200ull) __builtin_amdgcn_s_sleep(1);
    const u64 now = __builtin_amdgcn_s_memrealtime();
    u32 first = 0xffffffffu, mx = 0, mn = 0xffffffffu, sum = 0, cnt = 0;
    const u64 b0 = now - 4500ull;
    for (int w = lane; w < 1024; w += 64) {
      const u64 s0 = ld8(ws + STAMP_OFF + (w * NSTAMP + 0) * 8) - b0;
      const u64 sk = ld8(ws + STAMP_OFF + (w * NSTAMP + DIAGK) * 8) - b0;
      if (s0 <= 4500ull) first = min(first, (u32)s0);
      if (sk <= 4500ull) { mx = max(mx, (u32)sk); mn = min(mn, (u32)sk); sum += (u32)sk; ++cnt; }
    }
    for (int o = 1; o < 64; o <<= 1) { first = min(first, (u32)__shfl_xor((int)first, o)); mx = max(mx, (u32)__shfl_xor((int)mx, o)); mn = min(mn, (u32)__shfl_xor((int)mn, o)); sum += (u32)__shfl_xor((int)sum, o); cnt += (u32)__shfl_xor((int)cnt, o); }
    const u32 x = (DIAGMAX == 2 ? sum / max(cnt, 1u) : DIAGMAX ? mx : mn) - first;
    const u64 tend = b0 + first + 6000ull + (u64)(x * DIAGAMP);
    while (__builtin_amdgcn_s_memrealtime() < tend) __builtin_amdgcn_s_sleep(1);
  }
}

extern "C" void kernel_launch(void* const* d_in, const int* in_sizes, int n_in,
                              void* d_out, int out_size, void* d_ws, size_t ws_size,
                              hipStream_t stream) {
  rfa_main<<<NBH * NSPLIT, NWAVES * 64, 0, stream>>>((const char*)d_in[0], (const char*)d_in[1], (const char*)d_in[2],
                                                      (const char*)d_in[3], (char*)d_ws, (u32*)d_out);
}
```

```cpp
#include <hip/hip_runtime.h>
#include <hip/hip_bf16.h>
#include <stdint.h>

typedef unsigned int u32;
typedef unsigned long long u64;
using u32x2 = __attribute__((ext_vector_type(2))) unsigned;
using u32x4 = __attribute__((ext_vector_type(4))) unsigned;
using f32x2 = __attribute__((ext_vector_type(2))) float;
using f32x4 = __attribute__((ext_vector_type(4))) float;
using bf16x2 = __attribute__((ext_vector_type(2))) __bf16;
using bf16x8 = __attribute__((ext_vector_type(8))) __bf16;
using s16x4 = __attribute__((ext_vector_type(4))) short;
#define LAS __attribute__((address_space(3)))
#define GAS __attribute__((address_space(1)))

constexpr int NBH = 64, SK = 4096, D = 128, M = 8;
constexpr int NSW = 4;
constexpr int TILE_B = 4096;
constexpr int NSLOT = 8;
#define WIN 6
constexpr int NLD = 2;
constexpr int NWV = NSW + NLD + 1;
constexpr int AUX_B = 4096;
constexpr int WAVE_LDS = NSLOT * TILE_B + AUX_B;
constexpr int RED_OFF = NSW * WAVE_LDS, RED_STRIDE = 576;
constexpr int CW_OFF = RED_OFF + NSW * RED_STRIDE;
constexpr int STG_OFF = CW_OFF + 64;
constexpr int PB_OFF = STG_OFF + 3072, PB_B = 3072 + 64;
constexpr int LDS_BYTES = PB_OFF + 2 * PB_B;
constexpr size_t TSTEP = (size_t)NSW * TILE_B;
constexpr u32 WG_B = SK / 4 * D * 2;
constexpr int REC_B = 2048, HEAD_B = 4 * REC_B;
constexpr int OFF_DEN = 1024, OFF_BEACON = 3 * REC_B;

__device__ __forceinline__ float bf_lo(u32 x) { return __uint_as_float(x << 16); }
__device__ __forceinline__ float bf_hi(u32 x) { return __uint_as_float(x & 0xffff0000u); }
__device__ __forceinline__ u32 pk_bf16(float a, float b) {
  f32x2 f = {a, b};
  return __builtin_bit_cast(u32, __builtin_convertvector(f, bf16x2));
}
__device__ __forceinline__ u32 scale_pk(u32 x, float s) {
  f32x2 f = {bf_lo(x), bf_hi(x)};
  f = f * s;
  return __builtin_bit_cast(u32, __builtin_convertvector(f, bf16x2));
}
__device__ __forceinline__ float dot2(u32 a, u32 b, float c) {
  return __builtin_amdgcn_fdot2_f32_bf16(__builtin_bit_cast(bf16x2, a), __builtin_bit_cast(bf16x2, b), c, false);
}
__device__ __forceinline__ f32x4 mfma32(u32x4 a, u32x4 b, f32x4 c) {
  return __builtin_amdgcn_mfma_f32_16x16x32_bf16(__builtin_bit_cast(bf16x8, a), __builtin_bit_cast(bf16x8, b), c, 0, 0, 0);
}
__device__ __forceinline__ f32x4 mfma4f(float a, float b, f32x4 c) { return __builtin_amdgcn_mfma_f32_16x16x4f32(a, b, c, 0, 0, 0); }

__device__ __forceinline__ void fill4k(const char* sbase, const u32 (&voff)[4], u32 lds) {
  u32 keep;
  asm volatile("s_mov_b32 %0, m0\n\ts_mov_b32 m0, %6\n\ts_nop 0\n\t"
               "global_load_lds_dwordx4 %1, %5 offset:0 nt\n\t"
               "global_load_lds_dwordx4 %2, %5 offset:1024 nt\n\t"
               "global_load_lds_dwordx4 %3, %5 offset:2048 nt\n\t"
               "global_load_lds_dwordx4 %4, %5 offset:3072 nt\n\t"
               "s_mov_b32 m0, %0"
               : "=&s"(keep) : "v"(voff[0]), "v"(voff[1]), "v"(voff[2]), "v"(voff[3]), "s"(sbase), "s"(lds) : "memory");
}
__device__ __forceinline__ void dma1k(const char* sbase, u32 voff, u32 lds) {
  u32 keep;
  asm volatile("s_mov_b32 %0, m0\n\ts_mov_b32 m0, %3\n\ts_nop 0\n\t"
               "global_load_lds_dwordx4 %1, %2\n\t"
               "s_mov_b32 m0, %0"
               : "=&s"(keep) : "v"(voff), "s"(sbase), "s"(lds) : "memory");
}
__device__ __forceinline__ void dma1k_sc1(const char* sbase, u32 voff, u32 lds) {
  u32 keep;
  asm volatile("s_mov_b32 %0, m0\n\ts_mov_b32 m0, %3\n\ts_nop 0\n\t"
               "global_load_lds_dwordx4 %1, %2 sc1\n\t"
               "s_mov_b32 m0, %0"
               : "=&s"(keep) : "v"(voff), "s"(sbase), "s"(lds) : "memory");
}
template <int OFF> __device__ __forceinline__ u32x4 gld16(const char* sbase, u32 voff) {
  u32x4 r;
  asm volatile("global_load_dwordx4 %0, %1, %2 offset:%3" : "=v"(r) : "v"(voff), "s"(sbase), "i"(OFF) : "memory");
  return r;
}
template <int OFF> __device__ __forceinline__ u32x4 gld16_sc1(const char* sbase, u32 voff) {
  u32x4 r;
  asm volatile("global_load_dwordx4 %0, %1, %2 offset:%3 sc1" : "=v"(r) : "v"(voff), "s"(sbase), "i"(OFF) : "memory");
  return r;
}
__device__ __forceinline__ u32 lds_ld(u32 addr) { return __hip_atomic_load((LAS u32*)(size_t)addr, __ATOMIC_RELAXED, __HIP_MEMORY_SCOPE_WORKGROUP); }
__device__ __forceinline__ void lds_st(u32 addr, u32 v) { __hip_atomic_store((LAS u32*)(size_t)addr, v, __ATOMIC_RELAXED, __HIP_MEMORY_SCOPE_WORKGROUP); }
__device__ __forceinline__ u32 lds_add(u32 addr, u32 v) { return __hip_atomic_fetch_add((LAS u32*)(size_t)addr, v, __ATOMIC_RELAXED, __HIP_MEMORY_SCOPE_WORKGROUP); }
__device__ __forceinline__ u32 rfl(u32 x) { return __builtin_amdgcn_readfirstlane(x); }
__device__ __forceinline__ u64 now() { return __builtin_amdgcn_s_memrealtime(); }
#define OTOUCH 1
#define POLL_GAP 8
#define HINT_DN 1
#define BUMP_T 300ull
#define READ_T 1000ull
constexpr u64 TMO = 20000000ull;
__device__ __forceinline__ void st8(char* p, u32 lo, u32 hi) {
  __hip_atomic_store((GAS u64*)p, ((u64)hi << 32) | lo, __ATOMIC_RELAXED, __HIP_MEMORY_SCOPE_AGENT);
}
__device__ __forceinline__ void st4(char* p, u32 v) { __hip_atomic_store((GAS u32*)p, v, __ATOMIC_RELAXED, __HIP_MEMORY_SCOPE_AGENT); }
__device__ __forceinline__ u64 ld8(const char* p) { return __hip_atomic_load((GAS u64*)p, __ATOMIC_RELAXED, __HIP_MEMORY_SCOPE_AGENT); }
__device__ __forceinline__ u32 ld4(const char* p) { return __hip_atomic_load((GAS u32*)p, __ATOMIC_RELAXED, __HIP_MEMORY_SCOPE_AGENT); }
__device__ __forceinline__ float rbf(float x) { return bf_lo(pk_bf16(x, x)); }
template <int N> __device__ __forceinline__ void wait_vm() { asm volatile("s_waitcnt vmcnt(%0)" :: "i"(N) : "memory"); }
__device__ __forceinline__ void wait_lgkm0() { asm volatile("s_waitcnt lgkmcnt(0)" ::: "memory"); }
#define PIN4(x) asm volatile("" : "+v"(x[0]), "+v"(x[1]), "+v"(x[2]), "+v"(x[3]) :: "memory")
#define PIN8(x) asm volatile("" : "+v"(x[0]), "+v"(x[1]), "+v"(x[2]), "+v"(x[3]), "+v"(x[4]), "+v"(x[5]), "+v"(x[6]), "+v"(x[7]) :: "memory")
__device__ __forceinline__ u32x4 lds_rd128(u32 addr) { return *(const LAS u32x4*)(size_t)addr; }
__device__ __forceinline__ u32x2 lds_tr(u32 addr) {
  return __builtin_bit_cast(u32x2, __builtin_amdgcn_ds_read_tr16_b64_v4i16((LAS s16x4*)(size_t)addr));
}

__device__ __forceinline__ u32x2 phi4(f32x4 pr, f32x4 sq) {
  const u32 p01 = pk_bf16(pr[0], pr[1]), p23 = pk_bf16(pr[2], pr[3]);
  const u32 s01 = pk_bf16(sq[0], sq[1]), s23 = pk_bf16(sq[2], sq[3]);
  const u32 x01 = pk_bf16(fmaf(-0.5f, bf_lo(s01), bf_lo(p01)), fmaf(-0.5f, bf_hi(s01), bf_hi(p01)));
  const u32 x23 = pk_bf16(fmaf(-0.5f, bf_lo(s23), bf_lo(p23)), fmaf(-0.5f, bf_hi(s23), bf_hi(p23)));
  const float L2E = 1.4426950408889634f;
  const u32 e01 = pk_bf16(__builtin_amdgcn_exp2f(bf_lo(x01) * L2E), __builtin_amdgcn_exp2f(bf_hi(x01) * L2E));
  const u32 e23 = pk_bf16(__builtin_amdgcn_exp2f(bf_lo(x23) * L2E), __builtin_amdgcn_exp2f(bf_hi(x23) * L2E));
  const float R = 0.35355339059327373f;
  const u32 y01 = pk_bf16(bf_lo(e01) * R, bf_hi(e01) * R), y23 = pk_bf16(bf_lo(e23) * R, bf_hi(e23) * R);
  u32x2 o;
  o[0] = pk_bf16(bf_lo(y01) + 1e-6f, bf_hi(y01) + 1e-6f);
  o[1] = pk_bf16(bf_lo(y23) + 1e-6f, bf_hi(y23) + 1e-6f);
  return o;
}

#define STAMPV(k)
template <int R> __device__ __forceinline__ void drain(u32 word, u32& landed, int lane) {
  wait_vm<8 * R>();
  ++landed;
  if (lane == 0) lds_st(word, landed);
  if constexpr (R > 0) drain<R - 1>(word, landed, lane);
}
__device__ __forceinline__ u32 wait_ge(u32 word, u32 need) {
  u32 v = rfl(lds_ld(word));
  for (u32 it = 1; v < need; ++it) {
    v = rfl(lds_ld(word));
    if (it == (1u << 24)) break;
  }
  asm volatile("" ::: "memory");
  return v;
}
__global__ void __launch_bounds__(NWV * 64, 2)
rfa_main(const char* __restrict__ qg, const char* __restrict__ kg, const char* __restrict__ vg, const char* __restrict__ og,
         char* __restrict__ ws, u32* __restrict__ out) {
  __shared__ __attribute__((aligned(16))) char smem[LDS_BYTES];
  const int lane = threadIdx.x & 63, wave = __builtin_amdgcn_readfirstlane(threadIdx.x >> 6);
  const int g = lane >> 4, c = lane & 15;
  STAMPV(0);
  const u32 chunk = (blockIdx.x & 7) * 32 + (blockIdx.x >> 3);
  const u32 bh = chunk >> 2, split = chunk & 3;
  const u32 sm0 = (u32)(size_t)(LAS char*)smem;
  const u32 stg = sm0 + STG_OFF;
  const float SCALE = 0.29730177875068026f;
  u32 koff[4];
#pragma unroll
  for (int p = 0; p < 4; ++p) koff[p] = g * 256 + ((c ^ (4 * p + g)) << 4);
  const size_t hoff = (size_t)bh * (SK * D * 2) + split * WG_B + wave * (2 * TILE_B);
  asm volatile("" :: "s"(qg), "s"(kg), "s"(vg), "s"(og), "s"(ws), "s"(out), "v"(koff[0]), "v"(koff[1]), "v"(koff[2]), "v"(koff[3]), "s"(hoff));

  if (wave < NLD) {
    const char* kb = kg + hoff;
    const char* vb = vg + hoff;
    const u32 ring = sm0 + wave * (2 * WAVE_LDS);
    const u32 lw = sm0 + CW_OFF + 8 + wave * 4;
    if (wave == 0) { dma1k(og, lane * 16, stg); dma1k(og + 1024, lane * 16, stg + 1024); }
    else dma1k(qg + bh * (D * 2), c * 16, stg + 2048);
    fill4k(kb, koff, ring);
    fill4k(kb + TSTEP, koff, ring + TILE_B);
    if (lane == 0) lds_st(lw, 0u);
    asm volatile("s_waitcnt lgkmcnt(0)\n\ts_barrier" ::: "memory");
    __builtin_amdgcn_s_setprio(2);
    u32 voffs[4];
#pragma unroll
    for (int p = 0; p < 4; ++p) voffs[p] = g * 256 + ((c ^ (((4 * p + g) & 7) << 1)) << 4);
    u32 landed = 0, fr0 = 0, fr1 = 0;
#pragma unroll 1
    for (u32 n = 1; n < 32; ++n) {
      const u32 cw = n & 1, j = n >> 1;
      if (j >= 4) {
        if (cw == 0) { if (fr0 < j - 3) fr0 = wait_ge(sm0 + CW_OFF + 16 + wave * 8, j - 3); }
        else { if (fr1 < j - 3) fr1 = wait_ge(sm0 + CW_OFF + 20 + wave * 8, j - 3); }
      }
      const bool isk = j < 8;
      const char* src = (isk ? kb : vb) + cw * TILE_B + (j & 7) * (u32)(2 * TSTEP);
      u32 no[4];
#pragma unroll
      for (int i = 0; i < 4; ++i) no[i] = isk ? koff[i] : voffs[i];
      const u32 dst = ring + cw * WAVE_LDS + (j & 3) * (2 * TILE_B);
      fill4k(src, no, dst);
      fill4k(src + TSTEP, no, dst + TILE_B);
      if (n >= WIN - 1) {
        wait_vm<8 * (WIN - 1)>();
        ++landed;
        if (lane == 0) lds_st(lw, landed);
      }
    }
    STAMPV(1);
    drain<WIN - 2>(lw, landed, lane);
    STAMPV(4);
  } else if (wave == NWV - 1) {
    char* hrec = ws + (size_t)bh * HEAD_B;
    const bool owner = split == 0;
    u32x4 a0, a1, a2, a3;
    const u32 j3 = lane < 2 ? lane : 2;
    if (owner) {
      a0 = gld16_sc1<0>(hrec, lane * 16); a1 = gld16_sc1<REC_B>(hrec, lane * 16); a2 = gld16_sc1<0>(hrec + 2 * REC_B, lane * 16);
      a3 = gld16_sc1<OFF_DEN>(hrec, j3 * REC_B);
      dma1k((const char*)(out + bh * (D / 2)), c * 16, sm0 + PB_OFF);
    } else {
      a0 = gld16_sc1<0>(hrec + OFF_BEACON, 0u);
    }
    if (lane < 2) lds_st(sm0 + CW_OFF + lane * 4, 0u);
    const u64 t0 = now();
    asm volatile("s_waitcnt lgkmcnt(0)\n\ts_barrier" ::: "memory");
    {
      wait_ge(sm0 + CW_OFF + 8, 1); wait_ge(sm0 + CW_OFF + 12, 1);
      f32x4 qp = {0.f, 0.f, 0.f, 0.f};
      float qs = 0.f, qs2 = 0.f;
#pragma unroll
      for (int i = 0; i < 4; ++i) {
        const u32x4 om = lds_rd128(stg + (c & 7) * 256 + (4 * i + g) * 16);
        u32x4 qf = lds_rd128(stg + 2048 + (4 * i + g) * 16);
#pragma unroll
        for (int j = 0; j < 4; ++j) {
          qf[j] = scale_pk(qf[j], SCALE);
          if (j & 1) qs2 = dot2(qf[j], qf[j], qs2); else qs = dot2(qf[j], qf[j], qs);
        }
        qp = mfma32(om, qf, qp);
      }
      f32x4 qq = {0.f, 0.f, 0.f, 0.f};
      qq = mfma4f(1.0f, qs + qs2, qq);
      const u32x2 pq = phi4(qp, qq);
      if (c == 0 && g < 2) *(LAS f32x4*)(size_t)(sm0 + CW_OFF + 32 + g * 16) = f32x4{bf_lo(pq[0]), bf_hi(pq[0]), bf_lo(pq[1]), bf_hi(pq[1])};
      wait_lgkm0();
      if (lane == 0) lds_st(sm0 + CW_OFF + 4, 1u);
    }
    __builtin_amdgcn_s_setprio(3);
    u32 tag;
    float r0 = 0.f, r1 = 0.f, rd = 0.f, n0 = 0.f, n1 = 0.f, dsum = 0.f;
    bool have_local = false;
    auto sum_local = [&]() {
      asm volatile("" ::: "memory");
#pragma unroll
      for (int w = 0; w < NSW; ++w) {
        const f32x2 x = *(const LAS f32x2*)(size_t)(sm0 + RED_OFF + w * RED_STRIDE + lane * 8);
        n0 += x[0]; n1 += x[1]; dsum += *(const LAS float*)(size_t)(sm0 + RED_OFF + w * RED_STRIDE + 512);
      }
      have_local = true;
    };
    if (owner) {
      asm volatile("s_waitcnt vmcnt(0)" : "+v"(a0), "+v"(a1), "+v"(a2), "+v"(a3) :: "memory");
      u32 cand = rfl(a0[1]) + 1u;
      while (__any(a0[1] == cand || a0[3] == cand || a1[1] == cand || a1[3] == cand || a2[1] == cand || a2[3] == cand || a3[1] == cand)) ++cand;
      tag = cand;
      u32 seq = 1;
      if (lane == 0) st8(hrec + OFF_BEACON, seq, tag);
      STAMPV(1);
      u64 tb = now();
      auto bump = [&]() -> bool {
        const u64 tn = now();
        if (tn - tb > BUMP_T) { tb = tn; ++seq; if (lane == 0) st8(hrec + OFF_BEACON, seq, tag); }
        return tn - t0 > TMO;
      };
      while (rfl(lds_ld(sm0 + CW_OFF)) < HINT_DN) { __builtin_amdgcn_s_sleep(4); if (bump()) break; }
      auto poll_issue = [&](u32 buf) {
        dma1k_sc1(hrec, lane * 16, buf); dma1k_sc1(hrec + REC_B, lane * 16, buf + 1024); dma1k_sc1(hrec + 2 * REC_B, lane * 16, buf + 2048);
        if (lane < 3) dma1k_sc1(hrec + OFF_DEN, lane * REC_B, buf + 3072);
      };
      u32 cur = sm0 + PB_OFF, oth = cur + PB_B;
      poll_issue(cur);
      __builtin_amdgcn_s_sleep(POLL_GAP);
#pragma unroll 1
      for (u32 it = 1;; ++it) {
        poll_issue(oth);
        if (!have_local && rfl(lds_ld(sm0 + CW_OFF)) == NSW) sum_local();
        wait_vm<4>();
        a0 = lds_rd128(cur + lane * 16); a1 = lds_rd128(cur + 1024 + lane * 16); a2 = lds_rd128(cur + 2048 + lane * 16); a3 = lds_rd128(cur + 3072 + j3 * 16);
        if (__all(a0[1] == tag && a0[3] == tag && a1[1] == tag && a1[3] == tag && a2[1] == tag && a2[3] == tag && a3[1] == tag)) break;
        if ((it & 7) == 0 && bump()) break;
        const u32 x = cur; cur = oth; oth = x;
      }
      r0 = __uint_as_float(a0[0]) + __uint_as_float(a1[0]) + __uint_as_float(a2[0]);
      r1 = __uint_as_float(a0[2]) + __uint_as_float(a1[2]) + __uint_as_float(a2[2]);
      rd = __uint_as_float(__builtin_amdgcn_readlane(a3[0], 0)) + __uint_as_float(__builtin_amdgcn_readlane(a3[0], 1)) + __uint_as_float(__builtin_amdgcn_readlane(a3[0], 2));
    } else {
      asm volatile("s_waitcnt vmcnt(0)" : "+v"(a0) :: "memory");
      const u32 b0 = rfl(a0[0]), b1 = rfl(a0[1]);
      while (now() - t0 < READ_T && rfl(lds_ld(sm0 + CW_OFF)) == 0) __builtin_amdgcn_s_sleep(16);
      for (;;) {
        u64 x = 0;
        if (lane == 0) x = ld8(hrec + OFF_BEACON);
        tag = rfl((u32)(x >> 32));
        if (rfl((u32)x) != b0 || tag != b1 || now() - t0 > TMO) break;
        __builtin_amdgcn_s_sleep(32);
      }
      STAMPV(1);
    }
    STAMPV(3);
    if (!have_local) {
      for (u32 it = 1;; ++it) {
        const u32 dn = rfl(lds_ld(sm0 + CW_OFF));
        if (dn == NSW) break;
        if (dn == 0) __builtin_amdgcn_s_sleep(2);
        if ((it & 1023) == 0 && now() - t0 > TMO) break;
      }
      STAMPV(4);
      sum_local();
    }
    if (owner) {
      n0 += r0; n1 += r1; dsum += rd;
      const float den = rbf(rbf(dsum) + 1e-6f);
      out[bh * (D / 2) + lane] = pk_bf16(rbf(n0) / den, rbf(n1) / den);
      wait_vm<0>();
    } else {
      char* myrec = hrec + (split - 1) * REC_B;
      st8(myrec + lane * 16, __float_as_uint(n0), tag);
      st8(myrec + lane * 16 + 8, __float_as_uint(n1), tag);
      if (lane == 0) st8(myrec + OFF_DEN, __float_as_uint(dsum), tag);
    }
  } else {
    const u32 cwv = wave - NLD;
    const u32 lds0 = sm0 + cwv * WAVE_LDS;
    const u32 aux = lds0 + NSLOT * TILE_B;
    const u32 lw = sm0 + CW_OFF + 8 + (cwv >> 1) * 4, fw = sm0 + CW_OFF + 16 + cwv * 4;
    if (lane == 0) lds_st(fw, 0u);
    asm volatile("s_waitcnt lgkmcnt(0)\n\ts_barrier" ::: "memory");
    u32 kra[4], vra[8];
#pragma unroll
    for (int i = 0; i < 4; ++i) kra[i] = lds0 + c * 256 + (((4 * i + g) ^ c) << 4);
    {
      const u32 row = 4 * g + (c >> 2);
#pragma unroll
      for (int b = 0; b < 8; ++b) vra[b] = lds0 + row * 256 + ((b ^ (row & 7)) << 5) + (c & 3) * 8;
    }
    const float oneX = c < 8 ? 1.0f : 0.0f, oneY = c < 8 ? 0.0f : 1.0f;
    wait_ge(sm0 + CW_OFF + 8, 1);
    u32x4 omX[4], omY[4];
#pragma unroll
    for (int i = 0; i < 4; ++i) {
      const u32x4 om = lds_rd128(stg + (c & 7) * 256 + (4 * i + g) * 16);
#pragma unroll
      for (int j = 0; j < 4; ++j) { omX[i][j] = c < 8 ? om[j] : 0u; omY[i][j] = c < 8 ? 0u : om[j]; }
    }
    STAMPV(2);
    u32 seen = 0, need = (cwv & 1) + 1;
#pragma unroll 1
    for (u32 p = 0; p < 8; ++p) {
      const u32 so = (p & 3) * (2 * TILE_B);
      if (seen < need) seen = wait_ge(lw, need);
      need += 2;
      u32x4 kx[4], ky[4];
#pragma unroll
      for (int i = 0; i < 4; ++i) kx[i] = lds_rd128(kra[i] + so);
#pragma unroll
      for (int i = 0; i < 4; ++i) ky[i] = lds_rd128(kra[i] + so + TILE_B);
      wait_lgkm0();
      PIN4(kx); PIN4(ky);
      if (lane == 0) lds_st(fw, p + 1);
      f32x4 pr = {0.f, 0.f, 0.f, 0.f};
      float sx = 0.f, sy = 0.f, sx2 = 0.f, sy2 = 0.f;
#pragma unroll
      for (int i = 0; i < 4; ++i) {
#pragma unroll
        for (int j = 0; j < 4; ++j) { kx[i][j] = scale_pk(kx[i][j], SCALE); if (j & 1) sx2 = dot2(kx[i][j], kx[i][j], sx2); else sx = dot2(kx[i][j], kx[i][j], sx); }
        pr = mfma32(kx[i], omX[i], pr);
      }
#pragma unroll
      for (int i = 0; i < 4; ++i) {
#pragma unroll
        for (int j = 0; j < 4; ++j) { ky[i][j] = scale_pk(ky[i][j], SCALE); if (j & 1) sy2 = dot2(ky[i][j], ky[i][j], sy2); else sy = dot2(ky[i][j], ky[i][j], sy); }
        pr = mfma32(ky[i], omY[i], pr);
      }
      f32x4 sq = {0.f, 0.f, 0.f, 0.f};
      sq = mfma4f(sx + sx2, oneX, sq);
      sq = mfma4f(sy + sy2, oneY, sq);
      const u32x2 ph = phi4(pr, sq);
      *(LAS u32x2*)(size_t)(aux + p * 512 + lane * 8) = ph;
    }
    STAMPV(3);
    f32x4 acc[8], zacc = {0.f, 0.f, 0.f, 0.f};
#pragma unroll
    for (int b = 0; b < 8; ++b) acc[b] = f32x4{0.f, 0.f, 0.f, 0.f};
    const u32x4 ones = {0x3f803f80u, 0x3f803f80u, 0x3f803f80u, 0x3f803f80u};
    wait_ge(sm0 + CW_OFF + 4, 1);
    const f32x4 phq = *(const LAS f32x4*)(size_t)(sm0 + CW_OFF + 32 + (g & 1) * 16);
#pragma unroll 1
    for (u32 p = 0; p < 8; ++p) {
      const u32 so = (p & 3) * (2 * TILE_B);
      const u32x2 ph = *(const LAS u32x2*)(size_t)(aux + p * 512 + lane * 8);
      u32x4 a;
      a[0] = c < 8 ? ph[0] : 0u; a[1] = c < 8 ? ph[1] : 0u; a[2] = c < 8 ? 0u : ph[0]; a[3] = c < 8 ? 0u : ph[1];
      asm volatile("" : "+v"(a));
      if (seen < need) seen = wait_ge(lw, need);
      need += 2;
      u32x2 vx[8], vy[8];
#pragma unroll
      for (int b = 0; b < 8; ++b) vx[b] = lds_tr(vra[b] + so);
#pragma unroll
      for (int b = 0; b < 8; ++b) vy[b] = lds_tr(vra[b] + so + TILE_B);
      zacc = mfma32(a, ones, zacc);
      wait_lgkm0();
      PIN8(vx); PIN8(vy);
      if (lane == 0) lds_st(fw, p + 9);
#pragma unroll
      for (int b = 0; b < 8; ++b) {
        const u32x4 bb = {vx[b][0], vx[b][1], vy[b][0], vy[b][1]};
        acc[b] = mfma32(a, bb, acc[b]);
      }
    }
    STAMPV(4);
    float t[8];
#pragma unroll
    for (int b = 0; b < 8; ++b)
      t[b] = fmaf(phq[3], acc[b][3], fmaf(phq[2], acc[b][2], fmaf(phq[1], acc[b][1], phq[0] * acc[b][0])));
    float dn = fmaf(phq[3], zacc[3], fmaf(phq[2], zacc[2], fmaf(phq[1], zacc[1], phq[0] * zacc[0])));
    float u[4];
#pragma unroll
    for (int b = 0; b < 4; ++b) {
      auto s = __builtin_amdgcn_permlane32_swap(__float_as_uint(t[b]), __float_as_uint(t[b + 4]), false, false);
      u[b] = __uint_as_float(s[0]) + __uint_as_float(s[1]);
    }
    float w2[2];
#pragma unroll
    for (int b = 0; b < 2; ++b) {
      auto s = __builtin_amdgcn_permlane16_swap(__float_as_uint(u[b]), __float_as_uint(u[b + 2]), false, false);
      w2[b] = __uint_as_float(s[0]) + __uint_as_float(s[1]);
    }
    {
      auto s = __builtin_amdgcn_permlane32_swap(__float_as_uint(dn), __float_as_uint(dn), false, false);
      dn = __uint_as_float(s[0]) + __uint_as_float(s[1]);
      auto s2 = __builtin_amdgcn_permlane16_swap(__float_as_uint(dn), __float_as_uint(dn), false, false);
      dn = __uint_as_float(s2[0]) + __uint_as_float(s2[1]);
    }
    LAS float* red = (LAS float*)(size_t)(sm0 + RED_OFF + cwv * RED_STRIDE);
    red[32 * g + c] = w2[0];
    red[32 * g + 16 + c] = w2[1];
    if (lane == 0) red[128] = dn;
    wait_lgkm0();
    if (lane == 0) lds_add(sm0 + CW_OFF, 1u);
  }
}

extern "C" void kernel_launch(void* const* d_in, const int* in_sizes, int n_in,
                              void* d_out, int out_size, void* d_ws, size_t ws_size,
                              hipStream_t stream) {
  rfa_main<<<NBH * 4, NWV * 64, 0, stream>>>((const char*)d_in[0], (const char*)d_in[1], (const char*)d_in[2],
                                                  (const char*)d_in[3], (char*)d_ws, (u32*)d_out);
}
```

```cpp
#include <hip/hip_runtime.h>
#include <hip/hip_bf16.h>
#include <stdint.h>

typedef unsigned int u32;
typedef unsigned long long u64;
using u32x2 = __attribute__((ext_vector_type(2))) unsigned;
using u32x4 = __attribute__((ext_vector_type(4))) unsigned;
using f32x2 = __attribute__((ext_vector_type(2))) float;
using f32x4 = __attribute__((ext_vector_type(4))) float;
using bf16x2 = __attribute__((ext_vector_type(2))) __bf16;
using bf16x8 = __attribute__((ext_vector_type(8))) __bf16;
using s16x4 = __attribute__((ext_vector_type(4))) short;
#define LAS __attribute__((address_space(3)))
#define GAS __attribute__((address_space(1)))

constexpr int NBH = 64, SK = 4096, D = 128, M = 8;
constexpr int NSW = 4;
constexpr int TILE_B = 4096;
constexpr int NSLOT = 8;
#define WIN 6
constexpr int NLD = 2;
constexpr int NWV = NSW + NLD + 1;
constexpr int AUX_B = 4096;
constexpr int WAVE_LDS = NSLOT * TILE_B + AUX_B;
constexpr int RED_OFF = NSW * WAVE_LDS, RED_STRIDE = 576;
constexpr int CW_OFF = RED_OFF + NSW * RED_STRIDE;
constexpr int STG_OFF = CW_OFF + 64;
constexpr int PB_OFF = STG_OFF + 3072, PB_B = 4096 + 64;
constexpr int LDS_BYTES = PB_OFF + 2 * PB_B;
constexpr size_t TSTEP = (size_t)NSW * TILE_B;
constexpr u32 WG_B = SK / 4 * D * 2;
constexpr int REC_B = 2048, HEAD_B = 5 * REC_B;
constexpr int OFF_DEN = 1024, OFF_BEACON = 4 * REC_B, OFF_TICKET = 4 * REC_B + 1024;

__device__ __forceinline__ float bf_lo(u32 x) { return __uint_as_float(x << 16); }
__device__ __forceinline__ float bf_hi(u32 x) { return __uint_as_float(x & 0xffff0000u); }
__device__ __forceinline__ u32 pk_bf16(float a, float b) {
  f32x2 f = {a, b};
  return __builtin_bit_cast(u32, __builtin_convertvector(f, bf16x2));
}
__device__ __forceinline__ u32 scale_pk(u32 x, float s) {
  f32x2 f = {bf_lo(x), bf_hi(x)};
  f = f * s;
  return __builtin_bit_cast(u32, __builtin_convertvector(f, bf16x2));
}
__device__ __forceinline__ float dot2(u32 a, u32 b, float c) {
  return __builtin_amdgcn_fdot2_f32_bf16(__builtin_bit_cast(bf16x2, a), __builtin_bit_cast(bf16x2, b), c, false);
}
__device__ __forceinline__ f32x4 mfma32(u32x4 a, u32x4 b, f32x4 c) {
  return __builtin_amdgcn_mfma_f32_16x16x32_bf16(__builtin_bit_cast(bf16x8, a), __builtin_bit_cast(bf16x8, b), c, 0, 0, 0);
}
__device__ __forceinline__ f32x4 mfma4f(float a, float b, f32x4 c) { return __builtin_amdgcn_mfma_f32_16x16x4f32(a, b, c, 0, 0, 0); }

__device__ __forceinline__ void fill4k(const char* sbase, const u32 (&voff)[4], u32 lds) {
  u32 keep;
  asm volatile("s_mov_b32 %0, m0\n\ts_mov_b32 m0, %6\n\ts_nop 0\n\t"
               "global_load_lds_dwordx4 %1, %5 offset:0 nt\n\t"
               "global_load_lds_dwordx4 %2, %5 offset:1024 nt\n\t"
               "global_load_lds_dwordx4 %3, %5 offset:2048 nt\n\t"
               "global_load_lds_dwordx4 %4, %5 offset:3072 nt\n\t"
               "s_mov_b32 m0, %0"
               : "=&s"(keep) : "v"(voff[0]), "v"(voff[1]), "v"(voff[2]), "v"(voff[3]), "s"(sbase), "s"(lds) : "memory");
}
__device__ __forceinline__ void dma1k(const char* sbase, u32 voff, u32 lds) {
  u32 keep;
  asm volatile("s_mov_b32 %0, m0\n\ts_mov_b32 m0, %3\n\ts_nop 0\n\t"
               "global_load_lds_dwordx4 %1, %2\n\t"
               "s_mov_b32 m0, %0"
               : "=&s"(keep) : "v"(voff), "s"(sbase), "s"(lds) : "memory");
}
__device__ __forceinline__ void dma1k_sc1(const char* sbase, u32 voff, u32 lds) {
  u32 keep;
  asm volatile("s_mov_b32 %0, m0\n\ts_mov_b32 m0, %3\n\ts_nop 0\n\t"
               "global_load_lds_dwordx4 %1, %2 sc1\n\t"
               "s_mov_b32 m0, %0"
               : "=&s"(keep) : "v"(voff), "s"(sbase), "s"(lds) : "memory");
}
template <int OFF> __device__ __forceinline__ u32x4 gld16(const char* sbase, u32 voff) {
  u32x4 r;
  asm volatile("global_load_dwordx4 %0, %1, %2 offset:%3" : "=v"(r) : "v"(voff), "s"(sbase), "i"(OFF) : "memory");
  return r;
}
template <int OFF> __device__ __forceinline__ u32x4 gld16_sc1(const char* sbase, u32 voff) {
  u32x4 r;
  asm volatile("global_load_dwordx4 %0, %1, %2 offset:%3 sc1" : "=v"(r) : "v"(voff), "s"(sbase), "i"(OFF) : "memory");
  return r;
}
__device__ __forceinline__ u32 lds_ld(u32 addr) { return __hip_atomic_load((LAS u32*)(size_t)addr, __ATOMIC_RELAXED, __HIP_MEMORY_SCOPE_WORKGROUP); }
__device__ __forceinline__ void lds_st(u32 addr, u32 v) { __hip_atomic_store((LAS u32*)(size_t)addr, v, __ATOMIC_RELAXED, __HIP_MEMORY_SCOPE_WORKGROUP); }
__device__ __forceinline__ u32 lds_add(u32 addr, u32 v) { return __hip_atomic_fetch_add((LAS u32*)(size_t)addr, v, __ATOMIC_RELAXED, __HIP_MEMORY_SCOPE_WORKGROUP); }
__device__ __forceinline__ u32 rfl(u32 x) { return __builtin_amdgcn_readfirstlane(x); }
__device__ __forceinline__ u64 now() { return __builtin_amdgcn_s_memrealtime(); }
#define OTOUCH 1
#define POLL_GAP 8
#define TKN 22
#define MAPX 1
#define BUMP_T 300ull
#define READ_T 1000ull
constexpr u64 TMO = 20000000ull;
__device__ __forceinline__ void st8(char* p, u32 lo, u32 hi) {
  __hip_atomic_store((GAS u64*)p, ((u64)hi << 32) | lo, __ATOMIC_RELAXED, __HIP_MEMORY_SCOPE_AGENT);
}
__device__ __forceinline__ void st4(char* p, u32 v) { __hip_atomic_store((GAS u32*)p, v, __ATOMIC_RELAXED, __HIP_MEMORY_SCOPE_AGENT); }
__device__ __forceinline__ u64 ld8(const char* p) { return __hip_atomic_load((GAS u64*)p, __ATOMIC_RELAXED, __HIP_MEMORY_SCOPE_AGENT); }
__device__ __forceinline__ u32 ld4(const char* p) { return __hip_atomic_load((GAS u32*)p, __ATOMIC_RELAXED, __HIP_MEMORY_SCOPE_AGENT); }
__device__ __forceinline__ float rbf(float x) { return bf_lo(pk_bf16(x, x)); }
template <int N> __device__ __forceinline__ void wait_vm() { asm volatile("s_waitcnt vmcnt(%0)" :: "i"(N) : "memory"); }
__device__ __forceinline__ void wait_lgkm0() { asm volatile("s_waitcnt lgkmcnt(0)" ::: "memory"); }
#define PIN4(x) asm volatile("" : "+v"(x[0]), "+v"(x[1]), "+v"(x[2]), "+v"(x[3]) :: "memory")
#define PIN8(x) asm volatile("" : "+v"(x[0]), "+v"(x[1]), "+v"(x[2]), "+v"(x[3]), "+v"(x[4]), "+v"(x[5]), "+v"(x[6]), "+v"(x[7]) :: "memory")
__device__ __forceinline__ u32x4 lds_rd128(u32 addr) { return *(const LAS u32x4*)(size_t)addr; }
__device__ __forceinline__ u32x2 lds_tr(u32 addr) {
  return __builtin_bit_cast(u32x2, __builtin_amdgcn_ds_read_tr16_b64_v4i16((LAS s16x4*)(size_t)addr));
}

__device__ __forceinline__ u32x2 phi4(f32x4 pr, f32x4 sq) {
  const u32 p01 = pk_bf16(pr[0], pr[1]), p23 = pk_bf16(pr[2], pr[3]);
  const u32 s01 = pk_bf16(sq[0], sq[1]), s23 = pk_bf16(sq[2], sq[3]);
  const u32 x01 = pk_bf16(fmaf(-0.5f, bf_lo(s01), bf_lo(p01)), fmaf(-0.5f, bf_hi(s01), bf_hi(p01)));
  const u32 x23 = pk_bf16(fmaf(-0.5f, bf_lo(s23), bf_lo(p23)), fmaf(-0.5f, bf_hi(s23), bf_hi(p23)));
  const float L2E = 1.4426950408889634f;
  const u32 e01 = pk_bf16(__builtin_amdgcn_exp2f(bf_lo(x01) * L2E), __builtin_amdgcn_exp2f(bf_hi(x01) * L2E));
  const u32 e23 = pk_bf16(__builtin_amdgcn_exp2f(bf_lo(x23) * L2E), __builtin_amdgcn_exp2f(bf_hi(x23) * L2E));
  const float R = 0.35355339059327373f;
  const u32 y01 = pk_bf16(bf_lo(e01) * R, bf_hi(e01) * R), y23 = pk_bf16(bf_lo(e23) * R, bf_hi(e23) * R);
  u32x2 o;
  o[0] = pk_bf16(bf_lo(y01) + 1e-6f, bf_hi(y01) + 1e-6f);
  o[1] = pk_bf16(bf_lo(y23) + 1e-6f, bf_hi(y23) + 1e-6f);
  return o;
}

#define STAMPV(k)
template <int R> __device__ __forceinline__ void drain(u32 word, u32& landed, int lane) {
  wait_vm<8 * R>();
  ++landed;
  if (lane == 0) lds_st(word, landed);
  if constexpr (R > 0) drain<R - 1>(word, landed, lane);
}
__device__ __forceinline__ u32 wait_ge(u32 word, u32 need) {
  u32 v = rfl(lds_ld(word));
  for (u32 it = 1; v < need; ++it) {
    v = rfl(lds_ld(word));
    if (it == (1u << 24)) break;
  }
  asm volatile("" ::: "memory");
  return v;
}
__global__ void __launch_bounds__(NWV * 64, 2)
rfa_main(const char* __restrict__ qg, const char* __restrict__ kg, const char* __restrict__ vg, const char* __restrict__ og,
         char* __restrict__ ws, u32* __restrict__ out) {
  __shared__ __attribute__((aligned(16))) char smem[LDS_BYTES];
  const int lane = threadIdx.x & 63, wave = __builtin_amdgcn_readfirstlane(threadIdx.x >> 6);
  const int g = lane >> 4, c = lane & 15;
  STAMPV(0);
  const u32 bh = ((blockIdx.x >> 2) & 1) * 32 + (blockIdx.x >> 3), split = blockIdx.x & 3;
  const u32 sm0 = (u32)(size_t)(LAS char*)smem;
  const u32 stg = sm0 + STG_OFF;
  const float SCALE = 0.29730177875068026f;
  u32 koff[4];
#pragma unroll
  for (int p = 0; p < 4; ++p) koff[p] = g * 256 + ((c ^ (4 * p + g)) << 4);
  const size_t hoff = (size_t)bh * (SK * D * 2) + split * WG_B + wave * (2 * TILE_B);
  asm volatile("" :: "s"(qg), "s"(kg), "s"(vg), "s"(og), "s"(ws), "s"(out), "v"(koff[0]), "v"(koff[1]), "v"(koff[2]), "v"(koff[3]), "s"(hoff));

  if (wave < NLD) {
    const char* kb = kg + hoff;
    const char* vb = vg + hoff;
    const u32 ring = sm0 + wave * (2 * WAVE_LDS);
    const u32 lw = sm0 + CW_OFF + 8 + wave * 4;
    if (wave == 0) { dma1k(og, lane * 16, stg); dma1k(og + 1024, lane * 16, stg + 1024); }
    else dma1k(qg + bh * (D * 2), c * 16, stg + 2048);
    fill4k(kb, koff, ring);
    fill4k(kb + TSTEP, koff, ring + TILE_B);
    if (lane == 0) lds_st(lw, 0u);
    asm volatile("s_waitcnt lgkmcnt(0)\n\ts_barrier" ::: "memory");
    __builtin_amdgcn_s_setprio(2);
    u32 voffs[4];
#pragma unroll
    for (int p = 0; p < 4; ++p) voffs[p] = g * 256 + ((c ^ (((4 * p + g) & 7) << 1)) << 4);
    u32 landed = 0, fr0 = 0, fr1 = 0;
#pragma unroll 1
    for (u32 n = 1; n < 32; ++n) {
      const u32 cw = n & 1, j = n >> 1;
      if (j >= 4) {
        if (cw == 0) { if (fr0 < j - 3) fr0 = wait_ge(sm0 + CW_OFF + 16 + wave * 8, j - 3); }
        else { if (fr1 < j - 3) fr1 = wait_ge(sm0 + CW_OFF + 20 + wave * 8, j - 3); }
      }
      const bool isk = j < 8;
      const char* src = (isk ? kb : vb) + cw * TILE_B + (j & 7) * (u32)(2 * TSTEP);
      u32 no[4];
#pragma unroll
      for (int i = 0; i < 4; ++i) no[i] = isk ? koff[i] : voffs[i];
      const u32 dst = ring + cw * WAVE_LDS + (j & 3) * (2 * TILE_B);
      fill4k(src, no, dst);
      fill4k(src + TSTEP, no, dst + TILE_B);
      if (n >= WIN - 1) {
        wait_vm<8 * (WIN - 1)>();
        ++landed;
        if (lane == 0) lds_st(lw, landed);
      }
    }
    STAMPV(1);
    drain<WIN - 2>(lw, landed, lane);
    STAMPV(4);
  } else if (wave == NWV - 1) {
    char* hrec = ws + (size_t)bh * HEAD_B;
    const bool owner = split == 0;
    u32x4 a0, a1, a2, a3, ad;
    if (owner) {
      a0 = gld16_sc1<0>(hrec, lane * 16); a1 = gld16_sc1<REC_B>(hrec, lane * 16);
      a2 = gld16_sc1<0>(hrec + 2 * REC_B, lane * 16); a3 = gld16_sc1<REC_B>(hrec + 2 * REC_B, lane * 16);
      ad = gld16_sc1<OFF_DEN>(hrec, (lane & 3) * REC_B);
      if (lane == 0) st4(hrec + OFF_TICKET, 0u);
    } else {
      a0 = gld16_sc1<0>(hrec + OFF_BEACON, 0u);
    }
    if (lane < 2) lds_st(sm0 + CW_OFF + lane * 4, 0u);
    const u64 t0 = now();
    asm volatile("s_waitcnt lgkmcnt(0)\n\ts_barrier" ::: "memory");
    {
      wait_ge(sm0 + CW_OFF + 8, 1); wait_ge(sm0 + CW_OFF + 12, 1);
      f32x4 qp = {0.f, 0.f, 0.f, 0.f};
      float qs = 0.f, qs2 = 0.f;
#pragma unroll
      for (int i = 0; i < 4; ++i) {
        const u32x4 om = lds_rd128(stg + (c & 7) * 256 + (4 * i + g) * 16);
        u32x4 qf = lds_rd128(stg + 2048 + (4 * i + g) * 16);
#pragma unroll
        for (int j = 0; j < 4; ++j) {
          qf[j] = scale_pk(qf[j], SCALE);
          if (j & 1) qs2 = dot2(qf[j], qf[j], qs2); else qs = dot2(qf[j], qf[j], qs);
        }
        qp = mfma32(om, qf, qp);
      }
      f32x4 qq = {0.f, 0.f, 0.f, 0.f};
      qq = mfma4f(1.0f, qs + qs2, qq);
      const u32x2 pq = phi4(qp, qq);
      if (c == 0 && g < 2) *(LAS f32x4*)(size_t)(sm0 + CW_OFF + 32 + g * 16) = f32x4{bf_lo(pq[0]), bf_hi(pq[0]), bf_lo(pq[1]), bf_hi(pq[1])};
      wait_lgkm0();
      if (lane == 0) lds_st(sm0 + CW_OFF + 4, 1u);
    }
    __builtin_amdgcn_s_setprio(3);
    u32 tag, seq = 1;
    u64 tb = 0;
    float n0 = 0.f, n1 = 0.f, dsum = 0.f;
    bool have_local = false;
    auto sum_local = [&]() {
      asm volatile("" ::: "memory");
#pragma unroll
      for (int w = 0; w < NSW; ++w) {
        const f32x2 x = *(const LAS f32x2*)(size_t)(sm0 + RED_OFF + w * RED_STRIDE + lane * 8);
        n0 += x[0]; n1 += x[1]; dsum += *(const LAS float*)(size_t)(sm0 + RED_OFF + w * RED_STRIDE + 512);
      }
      have_local = true;
    };
    auto bump = [&]() -> bool {
      const u64 tn = now();
      if (owner && tn - tb > BUMP_T) { tb = tn; ++seq; if (lane == 0) st8(hrec + OFF_BEACON, seq, tag); }
      return tn - t0 > TMO;
    };
    if (owner) {
      asm volatile("s_waitcnt vmcnt(0)" : "+v"(a0), "+v"(a1), "+v"(a2), "+v"(a3), "+v"(ad) :: "memory");
      u32 cand = rfl(a0[1]) + 1u;
      while (__any(a0[1] == cand || a0[3] == cand || a1[1] == cand || a1[3] == cand || a2[1] == cand || a2[3] == cand ||
                   a3[1] == cand || a3[3] == cand || ad[1] == cand)) ++cand;
      tag = cand;
      if (lane == 0) st8(hrec + OFF_BEACON, seq, tag);
      tb = now();
    } else {
      asm volatile("s_waitcnt vmcnt(0)" : "+v"(a0) :: "memory");
      const u32 b0 = rfl(a0[0]), b1 = rfl(a0[1]);
      while (now() - t0 < READ_T && rfl(lds_ld(sm0 + CW_OFF + 8)) < TKN) __builtin_amdgcn_s_sleep(16);
      for (;;) {
        u64 x = 0;
        if (lane == 0) x = ld8(hrec + OFF_BEACON);
        tag = rfl((u32)(x >> 32));
        if (rfl((u32)x) != b0 || tag != b1 || now() - t0 > TMO) break;
        __builtin_amdgcn_s_sleep(32);
      }
    }
    STAMPV(1);
    while (rfl(lds_ld(sm0 + CW_OFF + 8)) < TKN || rfl(lds_ld(sm0 + CW_OFF + 12)) < TKN) { __builtin_amdgcn_s_sleep(4); if (bump()) break; }
    u32 tk = 0;
    if (lane == 0) tk = __hip_atomic_fetch_add((GAS u32*)(hrec + OFF_TICKET), 1u, __ATOMIC_RELAXED, __HIP_MEMORY_SCOPE_AGENT);
    const bool fin = rfl(tk) == 3u;
    STAMPV(2);
    if (fin) {
      const u32 j1 = (split + 1) & 3, j2 = (split + 2) & 3, j3 = (split + 3) & 3;
      auto poll_issue = [&](u32 buf) {
        dma1k_sc1(hrec + j1 * REC_B, lane * 16, buf + j1 * 1024); dma1k_sc1(hrec + j2 * REC_B, lane * 16, buf + j2 * 1024);
        dma1k_sc1(hrec + j3 * REC_B, lane * 16, buf + j3 * 1024);
        if (lane < 4) dma1k_sc1(hrec + OFF_DEN, lane * REC_B, buf + 4096);
      };
      u32 cur = sm0 + PB_OFF, oth = cur + PB_B;
      dma1k((const char*)(out + bh * (D / 2)), c * 16, cur + split * 1024);
      poll_issue(cur);
      __builtin_amdgcn_s_sleep(POLL_GAP);
#pragma unroll 1
      for (u32 it = 1;; ++it) {
        poll_issue(oth);
        if (!have_local && rfl(lds_ld(sm0 + CW_OFF)) == NSW) sum_local();
        wait_vm<4>();
        a0 = lds_rd128(cur + lane * 16); a1 = lds_rd128(cur + 1024 + lane * 16); a2 = lds_rd128(cur + 2048 + lane * 16); a3 = lds_rd128(cur + 3072 + lane * 16);
        ad = lds_rd128(cur + 4096 + (lane & 3) * 16);
        if (__all((split == 0 || (a0[1] == tag && a0[3] == tag)) && (split == 1 || (a1[1] == tag && a1[3] == tag)) &&
                  (split == 2 || (a2[1] == tag && a2[3] == tag)) && (split == 3 || (a3[1] == tag && a3[3] == tag)) &&
                  ((u32)(lane & 3) == split || ad[1] == tag))) break;
        if ((it & 7) == 0 && now() - t0 > TMO) break;
        const u32 x = cur; cur = oth; oth = x;
      }
    }
    STAMPV(3);
    bool all4 = !owner || fin;
    if (!have_local) {
      bool pending = false;
      const u32 land = sm0 + PB_OFF;
      for (u32 it = 1;; ++it) {
        const u32 dn = rfl(lds_ld(sm0 + CW_OFF));
        if (dn == NSW) break;
        if (!all4) {
          if (!pending) {
            if (lane == 0) { lds_st(land, ~0u); wait_lgkm0(); dma1k_sc1(hrec + OFF_TICKET, 0u, land); }
            pending = true;
          } else {
            const u32 x = rfl(lds_ld(land));
            if (x != ~0u) { pending = false; all4 = x == 4u; }
          }
          if (bump()) break;
        }
        if (dn == 0) __builtin_amdgcn_s_sleep(2);
        if ((it & 1023) == 0 && now() - t0 > TMO) break;
      }
      STAMPV(4);
      sum_local();
    }
    if (fin) {
      const float s0 = (((split == 0 ? n0 : __uint_as_float(a0[0])) + (split == 1 ? n0 : __uint_as_float(a1[0]))) +
                        (split == 2 ? n0 : __uint_as_float(a2[0]))) + (split == 3 ? n0 : __uint_as_float(a3[0]));
      const float s1 = (((split == 0 ? n1 : __uint_as_float(a0[2])) + (split == 1 ? n1 : __uint_as_float(a1[2]))) +
                        (split == 2 ? n1 : __uint_as_float(a2[2]))) + (split == 3 ? n1 : __uint_as_float(a3[2]));
      const u32 dl = (u32)(lane & 3) == split ? __float_as_uint(dsum) : ad[0];
      const float sd = ((__uint_as_float(__builtin_amdgcn_readlane(dl, 0)) + __uint_as_float(__builtin_amdgcn_readlane(dl, 1))) +
                        __uint_as_float(__builtin_amdgcn_readlane(dl, 2))) + __uint_as_float(__builtin_amdgcn_readlane(dl, 3));
      const float den = rbf(rbf(sd) + 1e-6f);
      out[bh * (D / 2) + lane] = pk_bf16(rbf(s0) / den, rbf(s1) / den);
      wait_vm<0>();
    } else {
      char* myrec = hrec + split * REC_B;
      st8(myrec + lane * 16, __float_as_uint(n0), tag);
      st8(myrec + lane * 16 + 8, __float_as_uint(n1), tag);
      if (lane == 0) st8(myrec + OFF_DEN, __float_as_uint(dsum), tag);
      if (!all4) {
        for (;;) {
          u32 x = 0;
          if (lane == 0) x = ld4(hrec + OFF_TICKET);
          if (rfl(x) == 4u || bump()) break;
          __builtin_amdgcn_s_sleep(8);
        }
      }
    }
  } else {
    const u32 cwv = wave - NLD;
    const u32 lds0 = sm0 + cwv * WAVE_LDS;
    const u32 aux = lds0 + NSLOT * TILE_B;
    const u32 lw = sm0 + CW_OFF + 8 + (cwv >> 1) * 4, fw = sm0 + CW_OFF + 16 + cwv * 4;
    if (lane == 0) lds_st(fw, 0u);
    asm volatile("s_waitcnt lgkmcnt(0)\n\ts_barrier" ::: "memory");
    u32 kra[4], vra[8];
#pragma unroll
    for (int i = 0; i < 4; ++i) kra[i] = lds0 + c * 256 + (((4 * i + g) ^ c) << 4);
    {
      const u32 row = 4 * g + (c >> 2);
#pragma unroll
      for (int b = 0; b < 8; ++b) vra[b] = lds0 + row * 256 + ((b ^ (row & 7)) << 5) + (c & 3) * 8;
    }
    const float oneX = c < 8 ? 1.0f : 0.0f, oneY = c < 8 ? 0.0f : 1.0f;
    wait_ge(sm0 + CW_OFF + 8, 1);
    u32x4 omX[4], omY[4];
#pragma unroll
    for (int i = 0; i < 4; ++i) {
      const u32x4 om = lds_rd128(stg + (c & 7) * 256 + (4 * i + g) * 16);
#pragma unroll
      for (int j = 0; j < 4; ++j) { omX[i][j] = c < 8 ? om[j] : 0u; omY[i][j] = c < 8 ? 0u : om[j]; }
    }
    STAMPV(2);
    u32 seen = 0, need = (cwv & 1) + 1;
#pragma unroll 1
    for (u32 p = 0; p < 8; ++p) {
      const u32 so = (p & 3) * (2 * TILE_B);
      if (seen < need) seen = wait_ge(lw, need);
      need += 2;
      u32x4 kx[4], ky[4];
#pragma unroll
      for (int i = 0; i < 4; ++i) kx[i] = lds_rd128(kra[i] + so);
#pragma unroll
      for (int i = 0; i < 4; ++i) ky[i] = lds_rd128(kra[i] + so + TILE_B);
      wait_lgkm0();
      PIN4(kx); PIN4(ky);
      if (lane == 0) lds_st(fw, p + 1);
      f32x4 pr = {0.f, 0.f, 0.f, 0.f};
      float sx = 0.f, sy = 0.f, sx2 = 0.f, sy2 = 0.f;
#pragma unroll
      for (int i = 0; i < 4; ++i) {
#pragma unroll
        for (int j = 0; j < 4; ++j) { kx[i][j] = scale_pk(kx[i][j], SCALE); if (j & 1) sx2 = dot2(kx[i][j], kx[i][j], sx2); else sx = dot2(kx[i][j], kx[i][j], sx); }
        pr = mfma32(kx[i], omX[i], pr);
      }
#pragma unroll
      for (int i = 0; i < 4; ++i) {
#pragma unroll
        for (int j = 0; j < 4; ++j) { ky[i][j] = scale_pk(ky[i][j], SCALE); if (j & 1) sy2 = dot2(ky[i][j], ky[i][j], sy2); else sy = dot2(ky[i][j], ky[i][j], sy); }
        pr = mfma32(ky[i], omY[i], pr);
      }
      f32x4 sq = {0.f, 0.f, 0.f, 0.f};
      sq = mfma4f(sx + sx2, oneX, sq);
      sq = mfma4f(sy + sy2, oneY, sq);
      const u32x2 ph = phi4(pr, sq);
      *(LAS u32x2*)(size_t)(aux + p * 512 + lane * 8) = ph;
    }
    STAMPV(3);
    f32x4 acc[8], zacc = {0.f, 0.f, 0.f, 0.f};
#pragma unroll
    for (int b = 0; b < 8; ++b) acc[b] = f32x4{0.f, 0.f, 0.f, 0.f};
    const u32x4 ones = {0x3f803f80u, 0x3f803f80u, 0x3f803f80u, 0x3f803f80u};
    wait_ge(sm0 + CW_OFF + 4, 1);
    const f32x4 phq = *(const LAS f32x4*)(size_t)(sm0 + CW_OFF + 32 + (g & 1) * 16);
#pragma unroll 1
    for (u32 p = 0; p < 8; ++p) {
      const u32 so = (p & 3) * (2 * TILE_B);
      const u32x2 ph = *(const LAS u32x2*)(size_t)(aux + p * 512 + lane * 8);
      u32x4 a;
      a[0] = c < 8 ? ph[0] : 0u; a[1] = c < 8 ? ph[1] : 0u; a[2] = c < 8 ? 0u : ph[0]; a[3] = c < 8 ? 0u : ph[1];
      asm volatile("" : "+v"(a));
      if (seen < need) seen = wait_ge(lw, need);
      need += 2;
      u32x2 vx[8], vy[8];
#pragma unroll
      for (int b = 0; b < 8; ++b) vx[b] = lds_tr(vra[b] + so);
#pragma unroll
      for (int b = 0; b < 8; ++b) vy[b] = lds_tr(vra[b] + so + TILE_B);
      zacc = mfma32(a, ones, zacc);
      wait_lgkm0();
      PIN8(vx); PIN8(vy);
      if (lane == 0) lds_st(fw, p + 9);
#pragma unroll
      for (int b = 0; b < 8; ++b) {
        const u32x4 bb = {vx[b][0], vx[b][1], vy[b][0], vy[b][1]};
        acc[b] = mfma32(a, bb, acc[b]);
      }
    }
    STAMPV(4);
    float t[8];
#pragma unroll
    for (int b = 0; b < 8; ++b)
      t[b] = fmaf(phq[3], acc[b][3], fmaf(phq[2], acc[b][2], fmaf(phq[1], acc[b][1], phq[0] * acc[b][0])));
    float dn = fmaf(phq[3], zacc[3], fmaf(phq[2], zacc[2], fmaf(phq[1], zacc[1], phq[0] * zacc[0])));
    float u[4];
#pragma unroll
    for (int b = 0; b < 4; ++b) {
      auto s = __builtin_amdgcn_permlane32_swap(__float_as_uint(t[b]), __float_as_uint(t[b + 4]), false, false);
      u[b] = __uint_as_float(s[0]) + __uint_as_float(s[1]);
    }
    float w2[2];
#pragma unroll
    for (int b = 0; b < 2; ++b) {
      auto s = __builtin_amdgcn_permlane16_swap(__float_as_uint(u[b]), __float_as_uint(u[b + 2]), false, false);
      w2[b] = __uint_as_float(s[0]) + __uint_as_float(s[1]);
    }
    {
      auto s = __builtin_amdgcn_permlane32_swap(__float_as_uint(dn), __float_as_uint(dn), false, false);
      dn = __uint_as_float(s[0]) + __uint_as_float(s[1]);
      auto s2 = __builtin_amdgcn_permlane16_swap(__float_as_uint(dn), __float_as_uint(dn), false, false);
      dn = __uint_as_float(s2[0]) + __uint_as_float(s2[1]);
    }
    LAS float* red = (LAS float*)(size_t)(sm0 + RED_OFF + cwv * RED_STRIDE);
    red[32 * g + c] = w2[0];
    red[32 * g + 16 + c] = w2[1];
    if (lane == 0) red[128] = dn;
    wait_lgkm0();
    if (lane == 0) lds_add(sm0 + CW_OFF, 1u);
  }
}

extern "C" void kernel_launch(void* const* d_in, const int* in_sizes, int n_in,
                              void* d_out, int out_size, void* d_ws, size_t ws_size,
                              hipStream_t stream) {
  rfa_main<<<NBH * 4, NWV * 64, 0, stream>>>((const char*)d_in[0], (const char*)d_in[1], (const char*)d_in[2],
                                                  (const char*)d_in[3], (char*)d_ws, (u32*)d_out);
}
```

```cpp
#include <hip/hip_runtime.h>
#include <hip/hip_bf16.h>
#include <stdint.h>

typedef unsigned int u32;
typedef unsigned long long u64;
using u32x2 = __attribute__((ext_vector_type(2))) unsigned;
using u32x4 = __attribute__((ext_vector_type(4))) unsigned;
using f32x2 = __attribute__((ext_vector_type(2))) float;
using f32x4 = __attribute__((ext_vector_type(4))) float;
using bf16x2 = __attribute__((ext_vector_type(2))) __bf16;
using bf16x8 = __attribute__((ext_vector_type(8))) __bf16;
using s16x4 = __attribute__((ext_vector_type(4))) short;
#define LAS __attribute__((address_space(3)))
#define GAS __attribute__((address_space(1)))

constexpr int NBH = 64, SK = 4096, D = 128, M = 8;
constexpr int NSW = 4;
constexpr int TILE_B = 4096;
constexpr int NSLOT = 8;
constexpr int WIN = 6;
constexpr int NLD = 2;
constexpr int NWV = NSW + NLD + 1;
constexpr int AUX_B = 4096;
constexpr int WAVE_LDS = NSLOT * TILE_B + AUX_B;
constexpr int RED_OFF = NSW * WAVE_LDS, RED_STRIDE = 576;
constexpr int CW_OFF = RED_OFF + NSW * RED_STRIDE;
constexpr int STG_OFF = CW_OFF + 64;
constexpr int PB_OFF = STG_OFF + 3072, PB_B = 4096 + 64;
constexpr int LDS_BYTES = PB_OFF + 2 * PB_B;
constexpr size_t TSTEP = (size_t)NSW * TILE_B;
constexpr u32 WG_B = SK / 4 * D * 2;
constexpr int REC_B = 2048, HEAD_B = 5 * REC_B;
constexpr int OFF_DEN = 1024, OFF_BEACON = 4 * REC_B, OFF_TICKET = 4 * REC_B + 1024;

__device__ __forceinline__ float bf_lo(u32 x) { return __uint_as_float(x << 16); }
__device__ __forceinline__ float bf_hi(u32 x) { return __uint_as_float(x & 0xffff0000u); }
__device__ __forceinline__ u32 pk_bf16(float a, float b) {
  f32x2 f = {a, b};
  return __builtin_bit_cast(u32, __builtin_convertvector(f, bf16x2));
}
__device__ __forceinline__ u32 scale_pk(u32 x, float s) {
  f32x2 f = {bf_lo(x), bf_hi(x)};
  f = f * s;
  return __builtin_bit_cast(u32, __builtin_convertvector(f, bf16x2));
}
__device__ __forceinline__ float dot2(u32 a, u32 b, float c) {
  return __builtin_amdgcn_fdot2_f32_bf16(__builtin_bit_cast(bf16x2, a), __builtin_bit_cast(bf16x2, b), c, false);
}
__device__ __forceinline__ f32x4 mfma32(u32x4 a, u32x4 b, f32x4 c) {
  return __builtin_amdgcn_mfma_f32_16x16x32_bf16(__builtin_bit_cast(bf16x8, a), __builtin_bit_cast(bf16x8, b), c, 0, 0, 0);
}
__device__ __forceinline__ f32x4 mfma4f(float a, float b, f32x4 c) { return __builtin_amdgcn_mfma_f32_16x16x4f32(a, b, c, 0, 0, 0); }

__device__ __forceinline__ void fill4k(const char* sbase, const u32 (&voff)[4], u32 lds) {
  u32 keep;
  asm volatile("s_mov_b32 %0, m0\n\ts_mov_b32 m0, %6\n\ts_nop 0\n\t"
               "global_load_lds_dwordx4 %1, %5 offset:0 nt\n\t"
               "global_load_lds_dwordx4 %2, %5 offset:1024 nt\n\t"
               "global_load_lds_dwordx4 %3, %5 offset:2048 nt\n\t"
               "global_load_lds_dwordx4 %4, %5 offset:3072 nt\n\t"
               "s_mov_b32 m0, %0"
               : "=&s"(keep) : "v"(voff[0]), "v"(voff[1]), "v"(voff[2]), "v"(voff[3]), "s"(sbase), "s"(lds) : "memory");
}
__device__ __forceinline__ void dma1k(const char* sbase, u32 voff, u32 lds) {
  u32 keep;
  asm volatile("s_mov_b32 %0, m0\n\ts_mov_b32 m0, %3\n\ts_nop 0\n\t"
               "global_load_lds_dwordx4 %1, %2\n\t"
               "s_mov_b32 m0, %0"
               : "=&s"(keep) : "v"(voff), "s"(sbase), "s"(lds) : "memory");
}
__device__ __forceinline__ void dma1k_sc1(const char* sbase, u32 voff, u32 lds) {
  u32 keep;
  asm volatile("s_mov_b32 %0, m0\n\ts_mov_b32 m0, %3\n\ts_nop 0\n\t"
               "global_load_lds_dwordx4 %1, %2 sc1\n\t"
               "s_mov_b32 m0, %0"
               : "=&s"(keep) : "v"(voff), "s"(sbase), "s"(lds) : "memory");
}
template <int OFF> __device__ __forceinline__ u32x4 gld16(const char* sbase, u32 voff) {
  u32x4 r;
  asm volatile("global_load_dwordx4 %0, %1, %2 offset:%3" : "=v"(r) : "v"(voff), "s"(sbase), "i"(OFF) : "memory");
  return r;
}
template <int OFF> __device__ __forceinline__ u32x4 gld16_sc1(const char* sbase, u32 voff) {
  u32x4 r;
  asm volatile("global_load_dwordx4 %0, %1, %2 offset:%3 sc1" : "=v"(r) : "v"(voff), "s"(sbase), "i"(OFF) : "memory");
  return r;
}
__device__ __forceinline__ u32 lds_ld(u32 addr) { return __hip_atomic_load((LAS u32*)(size_t)addr, __ATOMIC_RELAXED, __HIP_MEMORY_SCOPE_WORKGROUP); }
__device__ __forceinline__ void lds_st(u32 addr, u32 v) { __hip_atomic_store((LAS u32*)(size_t)addr, v, __ATOMIC_RELAXED, __HIP_MEMORY_SCOPE_WORKGROUP); }
__device__ __forceinline__ u32 lds_add(u32 addr, u32 v) { return __hip_atomic_fetch_add((LAS u32*)(size_t)addr, v, __ATOMIC_RELAXED, __HIP_MEMORY_SCOPE_WORKGROUP); }
__device__ __forceinline__ u32 rfl(u32 x) { return __builtin_amdgcn_readfirstlane(x); }
__device__ __forceinline__ u64 now() { return __builtin_amdgcn_s_memrealtime(); }
constexpr int POLL_GAP = 8;
constexpr u32 TKN = 22;
constexpr u64 BUMP_T = 300ull;
constexpr u64 READ_T = 1000ull;
constexpr u64 TMO = 20000000ull;
__device__ __forceinline__ void st8(char* p, u32 lo, u32 hi) {
  __hip_atomic_store((GAS u64*)p, ((u64)hi << 32) | lo, __ATOMIC_RELAXED, __HIP_MEMORY_SCOPE_AGENT);
}
__device__ __forceinline__ void st4(char* p, u32 v) { __hip_atomic_store((GAS u32*)p, v, __ATOMIC_RELAXED, __HIP_MEMORY_SCOPE_AGENT); }
__device__ __forceinline__ u64 ld8(const char* p) { return __hip_atomic_load((GAS u64*)p, __ATOMIC_RELAXED, __HIP_MEMORY_SCOPE_AGENT); }
__device__ __forceinline__ u32 ld4(const char* p) { return __hip_atomic_load((GAS u32*)p, __ATOMIC_RELAXED, __HIP_MEMORY_SCOPE_AGENT); }
__device__ __forceinline__ float rbf(float x) { return bf_lo(pk_bf16(x, x)); }
template <int N> __device__ __forceinline__ void wait_vm() { asm volatile("s_waitcnt vmcnt(%0)" :: "i"(N) : "memory"); }
__device__ __forceinline__ void wait_lgkm0() { asm volatile("s_waitcnt lgkmcnt(0)" ::: "memory"); }
#define PIN4(x) asm volatile("" : "+v"(x[0]), "+v"(x[1]), "+v"(x[2]), "+v"(x[3]) :: "memory")
#define PIN8(x) asm volatile("" : "+v"(x[0]), "+v"(x[1]), "+v"(x[2]), "+v"(x[3]), "+v"(x[4]), "+v"(x[5]), "+v"(x[6]), "+v"(x[7]) :: "memory")
__device__ __forceinline__ u32x4 lds_rd128(u32 addr) { return *(const LAS u32x4*)(size_t)addr; }
__device__ __forceinline__ u32x2 lds_tr(u32 addr) {
  return __builtin_bit_cast(u32x2, __builtin_amdgcn_ds_read_tr16_b64_v4i16((LAS s16x4*)(size_t)addr));
}

__device__ __forceinline__ u32x2 phi4(f32x4 pr, f32x4 sq) {
  const u32 p01 = pk_bf16(pr[0], pr[1]), p23 = pk_bf16(pr[2], pr[3]);
  const u32 s01 = pk_bf16(sq[0], sq[1]), s23 = pk_bf16(sq[2], sq[3]);
  const u32 x01 = pk_bf16(fmaf(-0.5f, bf_lo(s01), bf_lo(p01)), fmaf(-0.5f, bf_hi(s01), bf_hi(p01)));
  const u32 x23 = pk_bf16(fmaf(-0.5f, bf_lo(s23), bf_lo(p23)), fmaf(-0.5f, bf_hi(s23), bf_hi(p23)));
  const float L2E = 1.4426950408889634f;
  const u32 e01 = pk_bf16(__builtin_amdgcn_exp2f(bf_lo(x01) * L2E), __builtin_amdgcn_exp2f(bf_hi(x01) * L2E));
  const u32 e23 = pk_bf16(__builtin_amdgcn_exp2f(bf_lo(x23) * L2E), __builtin_amdgcn_exp2f(bf_hi(x23) * L2E));
  const float R = 0.35355339059327373f;
  const u32 y01 = pk_bf16(bf_lo(e01) * R, bf_hi(e01) * R), y23 = pk_bf16(bf_lo(e23) * R, bf_hi(e23) * R);
  u32x2 o;
  o[0] = pk_bf16(bf_lo(y01) + 1e-6f, bf_hi(y01) + 1e-6f);
  o[1] = pk_bf16(bf_lo(y23) + 1e-6f, bf_hi(y23) + 1e-6f);
  return o;
}

template <int R> __device__ __forceinline__ void drain(u32 word, u32& landed, int lane) {
  wait_vm<8 * R>();
  ++landed;
  if (lane == 0) lds_st(word, landed);
  if constexpr (R > 0) drain<R - 1>(word, landed, lane);
}
__device__ __forceinline__ u32 wait_ge(u32 word, u32 need) {
  u32 v = rfl(lds_ld(word));
  for (u32 it = 1; v < need; ++it) {
    v = rfl(lds_ld(word));
    if (it == (1u << 24)) break;
  }
  asm volatile("" ::: "memory");
  return v;
}
__global__ void __launch_bounds__(NWV * 64, 2)
rfa_main(const char* __restrict__ qg, const char* __restrict__ kg, const char* __restrict__ vg, const char* __restrict__ og,
         char* __restrict__ ws, u32* __restrict__ out) {
  __shared__ __attribute__((aligned(16))) char smem[LDS_BYTES];
  const int lane = threadIdx.x & 63, wave = __builtin_amdgcn_readfirstlane(threadIdx.x >> 6);
  const int g = lane >> 4, c = lane & 15;
  const u32 bh = ((blockIdx.x >> 2) & 1) * 32 + (blockIdx.x >> 3), split = blockIdx.x & 3;
  const u32 sm0 = (u32)(size_t)(LAS char*)smem;
  const u32 stg = sm0 + STG_OFF;
  const float SCALE = 0.29730177875068026f;
  u32 koff[4];
#pragma unroll
  for (int p = 0; p < 4; ++p) koff[p] = g * 256 + ((c ^ (4 * p + g)) << 4);
  const size_t hoff = (size_t)bh * (SK * D * 2) + split * WG_B + wave * (2 * TILE_B);
  asm volatile("" :: "s"(qg), "s"(kg), "s"(vg), "s"(og), "s"(ws), "s"(out), "v"(koff[0]), "v"(koff[1]), "v"(koff[2]), "v"(koff[3]), "s"(hoff));

  if (__builtin_expect(wave < NLD, 1)) {
    const char* kb = kg + hoff;
    const char* vb = vg + hoff;
    const u32 ring = sm0 + wave * (2 * WAVE_LDS);
    const u32 lw = sm0 + CW_OFF + 8 + wave * 4;
    if (wave == 0) { dma1k(og, lane * 16, stg); dma1k(og + 1024, lane * 16, stg + 1024); }
    else dma1k(qg + bh * (D * 2), c * 16, stg + 2048);
#pragma unroll
    for (int n = 0; n < WIN - 1; ++n) {
      fill4k(kb + (n & 1) * TILE_B + (n >> 1) * (2 * TSTEP), koff, ring + (n & 1) * WAVE_LDS + (n >> 1) * (2 * TILE_B));
      fill4k(kb + (n & 1) * TILE_B + (n >> 1) * (2 * TSTEP) + TSTEP, koff, ring + (n & 1) * WAVE_LDS + (n >> 1) * (2 * TILE_B) + TILE_B);
    }
    if (lane == 0) lds_st(lw, 0u);
    asm volatile("s_waitcnt lgkmcnt(0)\n\ts_barrier" ::: "memory");
    __builtin_amdgcn_s_setprio(2);
    u32 voffs[4];
#pragma unroll
    for (int p = 0; p < 4; ++p) voffs[p] = g * 256 + ((c ^ (((4 * p + g) & 7) << 1)) << 4);
    u32 landed = 0, fr0 = 0, fr1 = 0;
#pragma unroll 1
    for (u32 n = WIN - 1; n < 32; ++n) {
      const u32 cw = n & 1, j = n >> 1;
      if (j >= 4) {
        if (cw == 0) { if (fr0 < j - 3) fr0 = wait_ge(sm0 + CW_OFF + 16 + wave * 8, j - 3); }
        else { if (fr1 < j - 3) fr1 = wait_ge(sm0 + CW_OFF + 20 + wave * 8, j - 3); }
      }
      const bool isk = j < 8;
      const char* src = (isk ? kb : vb) + cw * TILE_B + (j & 7) * (u32)(2 * TSTEP);
      u32 no[4];
#pragma unroll
      for (int i = 0; i < 4; ++i) no[i] = isk ? koff[i] : voffs[i];
      const u32 dst = ring + cw * WAVE_LDS + (j & 3) * (2 * TILE_B);
      fill4k(src, no, dst);
      fill4k(src + TSTEP, no, dst + TILE_B);
      wait_vm<8 * (WIN - 1)>();
      ++landed;
      if (lane == 0) lds_st(lw, landed);
    }
    drain<WIN - 2>(lw, landed, lane);
  } else if (__builtin_expect(wave == NWV - 1, 1)) {
    char* hrec = ws + (size_t)bh * HEAD_B;
    const bool owner = split == 0;
    u32x4 a0, a1, a2, a3, ad;
    if (owner) {
      a0 = gld16_sc1<0>(hrec, lane * 16); a1 = gld16_sc1<REC_B>(hrec, lane * 16);
      a2 = gld16_sc1<0>(hrec + 2 * REC_B, lane * 16); a3 = gld16_sc1<REC_B>(hrec + 2 * REC_B, lane * 16);
      ad = gld16_sc1<OFF_DEN>(hrec, (lane & 3) * REC_B);
      if (lane == 0) st4(hrec + OFF_TICKET, 0u);
    } else {
      a0 = gld16_sc1<0>(hrec + OFF_BEACON, 0u);
    }
    if (lane < 2) lds_st(sm0 + CW_OFF + lane * 4, 0u);
    const u64 t0 = now();
    asm volatile("s_waitcnt lgkmcnt(0)\n\ts_barrier" ::: "memory");
    {
      wait_ge(sm0 + CW_OFF + 8, 1); wait_ge(sm0 + CW_OFF + 12, 1);
      f32x4 qp = {0.f, 0.f, 0.f, 0.f};
      float qs = 0.f, qs2 = 0.f;
#pragma unroll
      for (int i = 0; i < 4; ++i) {
        const u32x4 om = lds_rd128(stg + (c & 7) * 256 + (4 * i + g) * 16);
        u32x4 qf = lds_rd128(stg + 2048 + (4 * i + g) * 16);
#pragma unroll
        for (int j = 0; j < 4; ++j) {
          qf[j] = scale_pk(qf[j], SCALE);
          if (j & 1) qs2 = dot2(qf[j], qf[j], qs2); else qs = dot2(qf[j], qf[j], qs);
        }
        qp = mfma32(om, qf, qp);
      }
      f32x4 qq = {0.f, 0.f, 0.f, 0.f};
      qq = mfma4f(1.0f, qs + qs2, qq);
      const u32x2 pq = phi4(qp, qq);
      if (c == 0 && g < 2) *(LAS f32x4*)(size_t)(sm0 + CW_OFF + 32 + g * 16) = f32x4{bf_lo(pq[0]), bf_hi(pq[0]), bf_lo(pq[1]), bf_hi(pq[1])};
      wait_lgkm0();
      if (lane == 0) lds_st(sm0 + CW_OFF + 4, 1u);
    }
    __builtin_amdgcn_s_setprio(3);
    u32 tag, seq = 1;
    u64 tb = 0;
    float n0 = 0.f, n1 = 0.f, dsum = 0.f;
    bool have_local = false;
    auto sum_local = [&]() {
      asm volatile("" ::: "memory");
#pragma unroll
      for (int w = 0; w < NSW; ++w) {
        const f32x2 x = *(const LAS f32x2*)(size_t)(sm0 + RED_OFF + w * RED_STRIDE + lane * 8);
        n0 += x[0]; n1 += x[1]; dsum += *(const LAS float*)(size_t)(sm0 + RED_OFF + w * RED_STRIDE + 512);
      }
      have_local = true;
    };
    auto bump = [&]() -> bool {
      const u64 tn = now();
      if (owner && tn - tb > BUMP_T) { tb = tn; ++seq; if (lane == 0) st8(hrec + OFF_BEACON, seq, tag); }
      return tn - t0 > TMO;
    };
    if (owner) {
      asm volatile("s_waitcnt vmcnt(0)" : "+v"(a0), "+v"(a1), "+v"(a2), "+v"(a3), "+v"(ad) :: "memory");
      u32 cand = rfl(a0[1]) + 1u;
      while (__any(a0[1] == cand || a0[3] == cand || a1[1] == cand || a1[3] == cand || a2[1] == cand || a2[3] == cand ||
                   a3[1] == cand || a3[3] == cand || ad[1] == cand)) ++cand;
      tag = cand;
      if (lane == 0) st8(hrec + OFF_BEACON, seq, tag);
      tb = now();
    } else {
      asm volatile("s_waitcnt vmcnt(0)" : "+v"(a0) :: "memory");
      const u32 b0 = rfl(a0[0]), b1 = rfl(a0[1]);
      while (now() - t0 < READ_T && rfl(lds_ld(sm0 + CW_OFF + 8)) < TKN) __builtin_amdgcn_s_sleep(16);
      for (;;) {
        u64 x = 0;
        if (lane == 0) x = ld8(hrec + OFF_BEACON);
        tag = rfl((u32)(x >> 32));
        if (rfl((u32)x) != b0 || tag != b1 || now() - t0 > TMO) break;
        __builtin_amdgcn_s_sleep(32);
      }
    }
    while (rfl(lds_ld(sm0 + CW_OFF + 8)) < TKN || rfl(lds_ld(sm0 + CW_OFF + 12)) < TKN) { __builtin_amdgcn_s_sleep(4); if (bump()) break; }
    u32 tk = 0;
    if (lane == 0) tk = __hip_atomic_fetch_add((GAS u32*)(hrec + OFF_TICKET), 1u, __ATOMIC_RELAXED, __HIP_MEMORY_SCOPE_AGENT);
    const bool fin = rfl(tk) == 3u;
    if (fin) {
      const u32 j1 = (split + 1) & 3, j2 = (split + 2) & 3, j3 = (split + 3) & 3;
      auto poll_issue = [&](u32 buf) {
        dma1k_sc1(hrec + j1 * REC_B, lane * 16, buf + j1 * 1024); dma1k_sc1(hrec + j2 * REC_B, lane * 16, buf + j2 * 1024);
        dma1k_sc1(hrec + j3 * REC_B, lane * 16, buf + j3 * 1024);
        if (lane < 4) dma1k_sc1(hrec + OFF_DEN, lane * REC_B, buf + 4096);
      };
      u32 cur = sm0 + PB_OFF, oth = cur + PB_B;
      dma1k((const char*)(out + bh * (D / 2)), c * 16, cur + split * 1024);
      poll_issue(cur);
      __builtin_amdgcn_s_sleep(POLL_GAP);
#pragma unroll 1
      for (u32 it = 1;; ++it) {
        poll_issue(oth);
        if (!have_local && rfl(lds_ld(sm0 + CW_OFF)) == NSW) sum_local();
        wait_vm<4>();
        a0 = lds_rd128(cur + lane * 16); a1 = lds_rd128(cur + 1024 + lane * 16); a2 = lds_rd128(cur + 2048 + lane * 16); a3 = lds_rd128(cur + 3072 + lane * 16);
        ad = lds_rd128(cur + 4096 + (lane & 3) * 16);
        if (__all((split == 0 || (a0[1] == tag && a0[3] == tag)) && (split == 1 || (a1[1] == tag && a1[3] == tag)) &&
                  (split == 2 || (a2[1] == tag && a2[3] == tag)) && (split == 3 || (a3[1] == tag && a3[3] == tag)) &&
                  ((u32)(lane & 3) == split || ad[1] == tag))) break;
        if ((it & 7) == 0 && now() - t0 > TMO) break;
        const u32 x = cur; cur = oth; oth = x;
      }
    }
    bool all4 = !owner || fin;
    if (!have_local) {
      bool pending = false;
      const u32 land = sm0 + PB_OFF;
      for (u32 it = 1;; ++it) {
        const u32 dn = rfl(lds_ld(sm0 + CW_OFF));
        if (dn == NSW) break;
        if (!all4) {
          if (!pending) {
            if (lane == 0) { lds_st(land, ~0u); wait_lgkm0(); dma1k_sc1(hrec + OFF_TICKET, 0u, land); }
            pending = true;
          } else {
            const u32 x = rfl(lds_ld(land));
            if (x != ~0u) { pending = false; all4 = x == 4u; }
          }
          if (bump()) break;
        }
        if (dn == 0) __builtin_amdgcn_s_sleep(2);
        if ((it & 1023) == 0 && now() - t0 > TMO) break;
      }
      sum_local();
    }
    if (fin) {
      const float s0 = (((split == 0 ? n0 : __uint_as_float(a0[0])) + (split == 1 ? n0 : __uint_as_float(a1[0]))) +
                        (split == 2 ? n0 : __uint_as_float(a2[0]))) + (split == 3 ? n0 : __uint_as_float(a3[0]));
      const float s1 = (((split == 0 ? n1 : __uint_as_float(a0[2])) + (split == 1 ? n1 : __uint_as_float(a1[2]))) +
                        (split == 2 ? n1 : __uint_as_float(a2[2]))) + (split == 3 ? n1 : __uint_as_float(a3[2]));
      const u32 dl = (u32)(lane & 3) == split ? __float_as_uint(dsum) : ad[0];
      const float sd = ((__uint_as_float(__builtin_amdgcn_readlane(dl, 0)) + __uint_as_float(__builtin_amdgcn_readlane(dl, 1))) +
                        __uint_as_float(__builtin_amdgcn_readlane(dl, 2))) + __uint_as_float(__builtin_amdgcn_readlane(dl, 3));
      const float den = rbf(rbf(sd) + 1e-6f);
      out[bh * (D / 2) + lane] = pk_bf16(rbf(s0) / den, rbf(s1) / den);
      wait_vm<0>();
    } else {
      char* myrec = hrec + split * REC_B;
      st8(myrec + lane * 16, __float_as_uint(n0), tag);
      st8(myrec + lane * 16 + 8, __float_as_uint(n1), tag);
      if (lane == 0) st8(myrec + OFF_DEN, __float_as_uint(dsum), tag);
      if (!all4) {
        for (;;) {
          u32 x = 0;
          if (lane == 0) x = ld4(hrec + OFF_TICKET);
          if (rfl(x) == 4u || bump()) break;
          __builtin_amdgcn_s_sleep(8);
        }
      }
    }
  } else {
    const u32 cwv = wave - NLD;
    const u32 lds0 = sm0 + cwv * WAVE_LDS;
    const u32 aux = lds0 + NSLOT * TILE_B;
    const u32 lw = sm0 + CW_OFF + 8 + (cwv >> 1) * 4, fw = sm0 + CW_OFF + 16 + cwv * 4;
    if (lane == 0) lds_st(fw, 0u);
    asm volatile("s_waitcnt lgkmcnt(0)\n\ts_barrier" ::: "memory");
    u32 kra[4], vra[8];
#pragma unroll
    for (int i = 0; i < 4; ++i) kra[i] = lds0 + c * 256 + (((4 * i + g) ^ c) << 4);
    {
      const u32 row = 4 * g + (c >> 2);
#pragma unroll
      for (int b = 0; b < 8; ++b) vra[b] = lds0 + row * 256 + ((b ^ (row & 7)) << 5) + (c & 3) * 8;
    }
    const float oneX = c < 8 ? 1.0f : 0.0f, oneY = c < 8 ? 0.0f : 1.0f;
    wait_ge(sm0 + CW_OFF + 8, 1);
    u32x4 omX[4], omY[4];
#pragma unroll
    for (int i = 0; i < 4; ++i) {
      const u32x4 om = lds_rd128(stg + (c & 7) * 256 + (4 * i + g) * 16);
#pragma unroll
      for (int j = 0; j < 4; ++j) { omX[i][j] = c < 8 ? om[j] : 0u; omY[i][j] = c < 8 ? 0u : om[j]; }
    }
    u32 seen = 0, need = (cwv & 1) + 1;
#pragma unroll 1
    for (u32 p = 0; p < 8; ++p) {
      const u32 so = (p & 3) * (2 * TILE_B);
      if (seen < need) seen = wait_ge(lw, need);
      need += 2;
      u32x4 kx[4], ky[4];
#pragma unroll
      for (int i = 0; i < 4; ++i) kx[i] = lds_rd128(kra[i] + so);
#pragma unroll
      for (int i = 0; i < 4; ++i) ky[i] = lds_rd128(kra[i] + so + TILE_B);
      wait_lgkm0();
      PIN4(kx); PIN4(ky);
      if (lane == 0) lds_st(fw, p + 1);
      f32x4 pr = {0.f, 0.f, 0.f, 0.f};
      float sx = 0.f, sy = 0.f, sx2 = 0.f, sy2 = 0.f;
#pragma unroll
      for (int i = 0; i < 4; ++i) {
#pragma unroll
        for (int j = 0; j < 4; ++j) { kx[i][j] = scale_pk(kx[i][j], SCALE); if (j & 1) sx2 = dot2(kx[i][j], kx[i][j], sx2); else sx = dot2(kx[i][j], kx[i][j], sx); }
        pr = mfma32(kx[i], omX[i], pr);
      }
#pragma unroll
      for (int i = 0; i < 4; ++i) {
#pragma unroll
        for (int j = 0; j < 4; ++j) { ky[i][j] = scale_pk(ky[i][j], SCALE); if (j & 1) sy2 = dot2(ky[i][j], ky[i][j], sy2); else sy = dot2(ky[i][j], ky[i][j], sy); }
        pr = mfma32(ky[i], omY[i], pr);
      }
      f32x4 sq = {0.f, 0.f, 0.f, 0.f};
      sq = mfma4f(sx + sx2, oneX, sq);
      sq = mfma4f(sy + sy2, oneY, sq);
      const u32x2 ph = phi4(pr, sq);
      *(LAS u32x2*)(size_t)(aux + p * 512 + lane * 8) = ph;
    }
    f32x4 acc[8], zacc = {0.f, 0.f, 0.f, 0.f};
#pragma unroll
    for (int b = 0; b < 8; ++b) acc[b] = f32x4{0.f, 0.f, 0.f, 0.f};
    const u32x4 ones = {0x3f803f80u, 0x3f803f80u, 0x3f803f80u, 0x3f803f80u};
    wait_ge(sm0 + CW_OFF + 4, 1);
    const f32x4 phq = *(const LAS f32x4*)(size_t)(sm0 + CW_OFF + 32 + (g & 1) * 16);
#pragma unroll 1
    for (u32 p = 0; p < 8; ++p) {
      const u32 so = (p & 3) * (2 * TILE_B);
      const u32x2 ph = *(const LAS u32x2*)(size_t)(aux + p * 512 + lane * 8);
      u32x4 a;
      a[0] = c < 8 ? ph[0] : 0u; a[1] = c < 8 ? ph[1] : 0u; a[2] = c < 8 ? 0u : ph[0]; a[3] = c < 8 ? 0u : ph[1];
      asm volatile("" : "+v"(a));
      if (seen < need) seen = wait_ge(lw, need);
      need += 2;
      u32x2 vx[8], vy[8];
#pragma unroll
      for (int b = 0; b < 8; ++b) vx[b] = lds_tr(vra[b] + so);
#pragma unroll
      for (int b = 0; b < 8; ++b) vy[b] = lds_tr(vra[b] + so + TILE_B);
      zacc = mfma32(a, ones, zacc);
      wait_lgkm0();
      PIN8(vx); PIN8(vy);
      if (lane == 0) lds_st(fw, p + 9);
#pragma unroll
      for (int b = 0; b < 8; ++b) {
        const u32x4 bb = {vx[b][0], vx[b][1], vy[b][0], vy[b][1]};
        acc[b] = mfma32(a, bb, acc[b]);
      }
    }
    float t[8];
#pragma unroll
    for (int b = 0; b < 8; ++b)
      t[b] = fmaf(phq[3], acc[b][3], fmaf(phq[2], acc[b][2], fmaf(phq[1], acc[b][1], phq[0] * acc[b][0])));
    float dn = fmaf(phq[3], zacc[3], fmaf(phq[2], zacc[2], fmaf(phq[1], zacc[1], phq[0] * zacc[0])));
    float u[4];
#pragma unroll
    for (int b = 0; b < 4; ++b) {
      auto s = __builtin_amdgcn_permlane32_swap(__float_as_uint(t[b]), __float_as_uint(t[b + 4]), false, false);
      u[b] = __uint_as_float(s[0]) + __uint_as_float(s[1]);
    }
    float w2[2];
#pragma unroll
    for (int b = 0; b < 2; ++b) {
      auto s = __builtin_amdgcn_permlane16_swap(__float_as_uint(u[b]), __float_as_uint(u[b + 2]), false, false);
      w2[b] = __uint_as_float(s[0]) + __uint_as_float(s[1]);
    }
    {
      auto s = __builtin_amdgcn_permlane32_swap(__float_as_uint(dn), __float_as_uint(dn), false, false);
      dn = __uint_as_float(s[0]) + __uint_as_float(s[1]);
      auto s2 = __builtin_amdgcn_permlane16_swap(__float_as_uint(dn), __float_as_uint(dn), false, false);
      dn = __uint_as_float(s2[0]) + __uint_as_float(s2[1]);
    }
    LAS float* red = (LAS float*)(size_t)(sm0 + RED_OFF + cwv * RED_STRIDE);
    red[32 * g + c] = w2[0];
    red[32 * g + 16 + c] = w2[1];
    if (lane == 0) red[128] = dn;
    wait_lgkm0();
    if (lane == 0) lds_add(sm0 + CW_OFF, 1u);
  }
}

extern "C" void kernel_launch(void* const* d_in, const int* in_sizes, int n_in,
                              void* d_out, int out_size, void* d_ws, size_t ws_size,
                              hipStream_t stream) {
  rfa_main<<<NBH * 4, NWV * 64, 0, stream>>>((const char*)d_in[0], (const char*)d_in[1], (const char*)d_in[2],
                                                  (const char*)d_in[3], (char*)d_ws, (u32*)d_out);
}
```

```cpp
#include <hip/hip_runtime.h>
#include <hip/hip_bf16.h>
#include <stdint.h>

typedef unsigned int u32;
typedef unsigned long long u64;
using u32x2 = __attribute__((ext_vector_type(2))) unsigned;
using u32x4 = __attribute__((ext_vector_type(4))) unsigned;
using f32x2 = __attribute__((ext_vector_type(2))) float;
using f32x4 = __attribute__((ext_vector_type(4))) float;
using bf16x2 = __attribute__((ext_vector_type(2))) __bf16;
using bf16x8 = __attribute__((ext_vector_type(8))) __bf16;
using s16x4 = __attribute__((ext_vector_type(4))) short;
#define LAS __attribute__((address_space(3)))
#define GAS __attribute__((address_space(1)))

constexpr int NBH = 64, SK = 4096, D = 128, M = 8;
constexpr int NSW = 4;
constexpr int TILE_B = 4096;
constexpr int NSLOT = 8;
#define WIN 6
constexpr int NLD = 2;
constexpr int NWV = NSW + NLD + 1;
constexpr int AUX_B = 4096;
constexpr int WAVE_LDS = NSLOT * TILE_B + AUX_B;
constexpr int RED_OFF = NSW * WAVE_LDS, RED_STRIDE = 576;
constexpr int CW_OFF = RED_OFF + NSW * RED_STRIDE;
constexpr int STG_OFF = CW_OFF + 64;
constexpr int PB_OFF = STG_OFF + 3072, PB_B = 4096 + 64;
constexpr int LDS_BYTES = PB_OFF + 2 * PB_B;
constexpr size_t TSTEP = (size_t)NSW * TILE_B;
constexpr u32 WG_B = SK / 4 * D * 2;
constexpr int REC_B = 2048, HEAD_B = 5 * REC_B;
constexpr int OFF_DEN = 1024, OFF_BEACON = 4 * REC_B, OFF_TICKET = 4 * REC_B + 1024;

__device__ __forceinline__ float bf_lo(u32 x) { return __uint_as_float(x << 16); }
__device__ __forceinline__ float bf_hi(u32 x) { return __uint_as_float(x & 0xffff0000u); }
__device__ __forceinline__ u32 pk_bf16(float a, float b) {
  f32x2 f = {a, b};
  return __builtin_bit_cast(u32, __builtin_convertvector(f, bf16x2));
}
__device__ __forceinline__ u32 scale_pk(u32 x, float s) {
  f32x2 f = {bf_lo(x), bf_hi(x)};
  f = f * s;
  return __builtin_bit_cast(u32, __builtin_convertvector(f, bf16x2));
}
__device__ __forceinline__ float dot2(u32 a, u32 b, float c) {
  return __builtin_amdgcn_fdot2_f32_bf16(__builtin_bit_cast(bf16x2, a), __builtin_bit_cast(bf16x2, b), c, false);
}
__device__ __forceinline__ f32x4 mfma32(u32x4 a, u32x4 b, f32x4 c) {
  return __builtin_amdgcn_mfma_f32_16x16x32_bf16(__builtin_bit_cast(bf16x8, a), __builtin_bit_cast(bf16x8, b), c, 0, 0, 0);
}
__device__ __forceinline__ f32x4 mfma4f(float a, float b, f32x4 c) { return __builtin_amdgcn_mfma_f32_16x16x4f32(a, b, c, 0, 0, 0); }

__device__ __forceinline__ void fill4k(const char* sbase, const u32 (&voff)[4], u32 lds) {
  u32 keep;
  asm volatile("s_mov_b32 %0, m0\n\ts_mov_b32 m0, %6\n\ts_nop 0\n\t"
               "global_load_lds_dwordx4 %1, %5 offset:0 nt\n\t"
               "global_load_lds_dwordx4 %2, %5 offset:1024 nt\n\t"
               "global_load_lds_dwordx4 %3, %5 offset:2048 nt\n\t"
               "global_load_lds_dwordx4 %4, %5 offset:3072 nt\n\t"
               "s_mov_b32 m0, %0"
               : "=&s"(keep) : "v"(voff[0]), "v"(voff[1]), "v"(voff[2]), "v"(voff[3]), "s"(sbase), "s"(lds) : "memory");
}
__device__ __forceinline__ void dma1k(const char* sbase, u32 voff, u32 lds) {
  u32 keep;
  asm volatile("s_mov_b32 %0, m0\n\ts_mov_b32 m0, %3\n\ts_nop 0\n\t"
               "global_load_lds_dwordx4 %1, %2\n\t"
               "s_mov_b32 m0, %0"
               : "=&s"(keep) : "v"(voff), "s"(sbase), "s"(lds) : "memory");
}
__device__ __forceinline__ void dma1k_sc1(const char* sbase, u32 voff, u32 lds) {
  u32 keep;
  asm volatile("s_mov_b32 %0, m0\n\ts_mov_b32 m0, %3\n\ts_nop 0\n\t"
               "global_load_lds_dwordx4 %1, %2 sc1\n\t"
               "s_mov_b32 m0, %0"
               : "=&s"(keep) : "v"(voff), "s"(sbase), "s"(lds) : "memory");
}
template <int OFF> __device__ __forceinline__ u32x4 gld16(const char* sbase, u32 voff) {
  u32x4 r;
  asm volatile("global_load_dwordx4 %0, %1, %2 offset:%3" : "=v"(r) : "v"(voff), "s"(sbase), "i"(OFF) : "memory");
  return r;
}
template <int OFF> __device__ __forceinline__ u32x4 gld16_sc1(const char* sbase, u32 voff) {
  u32x4 r;
  asm volatile("global_load_dwordx4 %0, %1, %2 offset:%3 sc1" : "=v"(r) : "v"(voff), "s"(sbase), "i"(OFF) : "memory");
  return r;
}
__device__ __forceinline__ u32 lds_ld(u32 addr) { return __hip_atomic_load((LAS u32*)(size_t)addr, __ATOMIC_RELAXED, __HIP_MEMORY_SCOPE_WORKGROUP); }
__device__ __forceinline__ void lds_st(u32 addr, u32 v) { __hip_atomic_store((LAS u32*)(size_t)addr, v, __ATOMIC_RELAXED, __HIP_MEMORY_SCOPE_WORKGROUP); }
__device__ __forceinline__ u32 lds_add(u32 addr, u32 v) { return __hip_atomic_fetch_add((LAS u32*)(size_t)addr, v, __ATOMIC_RELAXED, __HIP_MEMORY_SCOPE_WORKGROUP); }
__device__ __forceinline__ u32 rfl(u32 x) { return __builtin_amdgcn_readfirstlane(x); }
__device__ __forceinline__ u64 now() { return __builtin_amdgcn_s_memrealtime(); }
#define OTOUCH 1
#define POLL_GAP 8
#define TKN 22
#define MAPX 1
#define BUMP_T 300ull
#define READ_T 1000ull
constexpr u64 TMO = 20000000ull;
__device__ __forceinline__ void st8(char* p, u32 lo, u32 hi) {
  __hip_atomic_store((GAS u64*)p, ((u64)hi << 32) | lo, __ATOMIC_RELAXED, __HIP_MEMORY_SCOPE_AGENT);
}
__device__ __forceinline__ void st4(char* p, u32 v) { __hip_atomic_store((GAS u32*)p, v, __ATOMIC_RELAXED, __HIP_MEMORY_SCOPE_AGENT); }
__device__ __forceinline__ u64 ld8(const char* p) { return __hip_atomic_load((GAS u64*)p, __ATOMIC_RELAXED, __HIP_MEMORY_SCOPE_AGENT); }
__device__ __forceinline__ u32 ld4(const char* p) { return __hip_atomic_load((GAS u32*)p, __ATOMIC_RELAXED, __HIP_MEMORY_SCOPE_AGENT); }
__device__ __forceinline__ float rbf(float x) { return bf_lo(pk_bf16(x, x)); }
template <int N> __device__ __forceinline__ void wait_vm() { asm volatile("s_waitcnt vmcnt(%0)" :: "i"(N) : "memory"); }
__device__ __forceinline__ void wait_lgkm0() { asm volatile("s_waitcnt lgkmcnt(0)" ::: "memory"); }
#define PIN4(x) asm volatile("" : "+v"(x[0]), "+v"(x[1]), "+v"(x[2]), "+v"(x[3]) :: "memory")
#define PIN8(x) asm volatile("" : "+v"(x[0]), "+v"(x[1]), "+v"(x[2]), "+v"(x[3]), "+v"(x[4]), "+v"(x[5]), "+v"(x[6]), "+v"(x[7]) :: "memory")
__device__ __forceinline__ u32x4 lds_rd128(u32 addr) { return *(const LAS u32x4*)(size_t)addr; }
__device__ __forceinline__ u32x2 lds_tr(u32 addr) {
  return __builtin_bit_cast(u32x2, __builtin_amdgcn_ds_read_tr16_b64_v4i16((LAS s16x4*)(size_t)addr));
}

__device__ __forceinline__ u32x2 phi4(f32x4 pr, f32x4 sq) {
  const u32 p01 = pk_bf16(pr[0], pr[1]), p23 = pk_bf16(pr[2], pr[3]);
  const u32 s01 = pk_bf16(sq[0], sq[1]), s23 = pk_bf16(sq[2], sq[3]);
  const u32 x01 = pk_bf16(fmaf(-0.5f, bf_lo(s01), bf_lo(p01)), fmaf(-0.5f, bf_hi(s01), bf_hi(p01)));
  const u32 x23 = pk_bf16(fmaf(-0.5f, bf_lo(s23), bf_lo(p23)), fmaf(-0.5f, bf_hi(s23), bf_hi(p23)));
  const float L2E = 1.4426950408889634f;
  const u32 e01 = pk_bf16(__builtin_amdgcn_exp2f(bf_lo(x01) * L2E), __builtin_amdgcn_exp2f(bf_hi(x01) * L2E));
  const u32 e23 = pk_bf16(__builtin_amdgcn_exp2f(bf_lo(x23) * L2E), __builtin_amdgcn_exp2f(bf_hi(x23) * L2E));
  const float R = 0.35355339059327373f;
  const u32 y01 = pk_bf16(bf_lo(e01) * R, bf_hi(e01) * R), y23 = pk_bf16(bf_lo(e23) * R, bf_hi(e23) * R);
  u32x2 o;
  o[0] = pk_bf16(bf_lo(y01) + 1e-6f, bf_hi(y01) + 1e-6f);
  o[1] = pk_bf16(bf_lo(y23) + 1e-6f, bf_hi(y23) + 1e-6f);
  return o;
}

#define STAMPV(k)
template <int R> __device__ __forceinline__ void drain(u32 word, u32& landed, int lane) {
  wait_vm<8 * R>();
  ++landed;
  if (lane == 0) lds_st(word, landed);
  if constexpr (R > 0) drain<R - 1>(word, landed, lane);
}
__device__ __forceinline__ u32 wait_ge(u32 word, u32 need) {
  u32 v = rfl(lds_ld(word));
  for (u32 it = 1; v < need; ++it) {
    v = rfl(lds_ld(word));
    if (it == (1u << 24)) break;
  }
  asm volatile("" ::: "memory");
  return v;
}
__global__ void __launch_bounds__(NWV * 64, 2)
rfa_main(const char* __restrict__ qg, const char* __restrict__ kg, const char* __restrict__ vg, const char* __restrict__ og,
         char* __restrict__ ws, u32* __restrict__ out) {
  __shared__ __attribute__((aligned(16))) char smem[LDS_BYTES];
  const int lane = threadIdx.x & 63, wave = __builtin_amdgcn_readfirstlane(threadIdx.x >> 6);
  const int g = lane >> 4, c = lane & 15;
  STAMPV(0);
  const u32 bh = ((blockIdx.x >> 2) & 1) * 32 + (blockIdx.x >> 3), split = blockIdx.x & 3;
  const u32 sm0 = (u32)(size_t)(LAS char*)smem;
  const u32 stg = sm0 + STG_OFF;
  const float SCALE = 0.29730177875068026f;
  u32 koff[4];
#pragma unroll
  for (int p = 0; p < 4; ++p) koff[p] = g * 256 + ((c ^ (4 * p + g)) << 4);
  const size_t hoff = (size_t)bh * (SK * D * 2) + split * WG_B + wave * (2 * TILE_B);
  asm volatile("" :: "s"(qg), "s"(kg), "s"(vg), "s"(og), "s"(ws), "s"(out), "v"(koff[0]), "v"(koff[1]), "v"(koff[2]), "v"(koff[3]), "s"(hoff));

  if (__builtin_expect(wave < NLD, 1)) {
    const char* kb = kg + hoff;
    const char* vb = vg + hoff;
    const u32 ring = sm0 + wave * (2 * WAVE_LDS);
    const u32 lw = sm0 + CW_OFF + 8 + wave * 4;
    if (wave == 0) { dma1k(og, lane * 16, stg); dma1k(og + 1024, lane * 16, stg + 1024); }
    else dma1k(qg + bh * (D * 2), c * 16, stg + 2048);
#pragma unroll
    for (int n = 0; n < WIN - 1; ++n) {
      fill4k(kb + (n & 1) * TILE_B + (n >> 1) * (2 * TSTEP), koff, ring + (n & 1) * WAVE_LDS + (n >> 1) * (2 * TILE_B));
      fill4k(kb + (n & 1) * TILE_B + (n >> 1) * (2 * TSTEP) + TSTEP, koff, ring + (n & 1) * WAVE_LDS + (n >> 1) * (2 * TILE_B) + TILE_B);
    }
    if (lane == 0) lds_st(lw, 0u);
    asm volatile("s_waitcnt lgkmcnt(0)\n\ts_barrier" ::: "memory");
    __builtin_amdgcn_s_setprio(2);
    u32 voffs[4];
#pragma unroll
    for (int p = 0; p < 4; ++p) voffs[p] = g * 256 + ((c ^ (((4 * p + g) & 7) << 1)) << 4);
    u32 landed = 0, fr0 = 0, fr1 = 0;
#pragma unroll 1
    for (u32 n = WIN - 1; n < 32; ++n) {
      const u32 cw = n & 1, j = n >> 1;
      if (j >= 4) {
        if (cw == 0) { if (fr0 < j - 3) fr0 = wait_ge(sm0 + CW_OFF + 16 + wave * 8, j - 3); }
        else { if (fr1 < j - 3) fr1 = wait_ge(sm0 + CW_OFF + 20 + wave * 8, j - 3); }
      }
      const bool isk = j < 8;
      const char* src = (isk ? kb : vb) + cw * TILE_B + (j & 7) * (u32)(2 * TSTEP);
      u32 no[4];
#pragma unroll
      for (int i = 0; i < 4; ++i) no[i] = isk ? koff[i] : voffs[i];
      const u32 dst = ring + cw * WAVE_LDS + (j & 3) * (2 * TILE_B);
      fill4k(src, no, dst);
      fill4k(src + TSTEP, no, dst + TILE_B);
      wait_vm<8 * (WIN - 1)>();
      ++landed;
      if (lane == 0) lds_st(lw, landed);
    }
    STAMPV(1);
    drain<WIN - 2>(lw, landed, lane);
    STAMPV(4);
  } else if (__builtin_expect(wave == NWV - 1, 1)) {
    char* hrec = ws + (size_t)bh * HEAD_B;
    const bool owner = split == 0;
    u32x4 a0, a1, a2, a3, ad;
    if (owner) {
      a0 = gld16_sc1<0>(hrec, lane * 16); a1 = gld16_sc1<REC_B>(hrec, lane * 16);
      a2 = gld16_sc1<0>(hrec + 2 * REC_B, lane * 16); a3 = gld16_sc1<REC_B>(hrec + 2 * REC_B, lane * 16);
      ad = gld16_sc1<OFF_DEN>(hrec, (lane & 3) * REC_B);
      if (lane == 0) st4(hrec + OFF_TICKET, 0u);
    } else {
      a0 = gld16_sc1<0>(hrec + OFF_BEACON, 0u);
    }
    if (lane < 2) lds_st(sm0 + CW_OFF + lane * 4, 0u);
    const u64 t0 = now();
    asm volatile("s_waitcnt lgkmcnt(0)\n\ts_barrier" ::: "memory");
    {
      wait_ge(sm0 + CW_OFF + 8, 1); wait_ge(sm0 + CW_OFF + 12, 1);
      f32x4 qp = {0.f, 0.f, 0.f, 0.f};
      float qs = 0.f, qs2 = 0.f;
#pragma unroll
      for (int i = 0; i < 4; ++i) {
        const u32x4 om = lds_rd128(stg + (c & 7) * 256 + (4 * i + g) * 16);
        u32x4 qf = lds_rd128(stg + 2048 + (4 * i + g) * 16);
#pragma unroll
        for (int j = 0; j < 4; ++j) {
          qf[j] = scale_pk(qf[j], SCALE);
          if (j & 1) qs2 = dot2(qf[j], qf[j], qs2); else qs = dot2(qf[j], qf[j], qs);
        }
        qp = mfma32(om, qf, qp);
      }
      f32x4 qq = {0.f, 0.f, 0.f, 0.f};
      qq = mfma4f(1.0f, qs + qs2, qq);
      const u32x2 pq = phi4(qp, qq);
      if (c == 0 && g < 2) *(LAS f32x4*)(size_t)(sm0 + CW_OFF + 32 + g * 16) = f32x4{bf_lo(pq[0]), bf_hi(pq[0]), bf_lo(pq[1]), bf_hi(pq[1])};
      wait_lgkm0();
      if (lane == 0) lds_st(sm0 + CW_OFF + 4, 1u);
    }
    __builtin_amdgcn_s_setprio(3);
    u32 tag, seq = 1;
    u64 tb = 0;
    float n0 = 0.f, n1 = 0.f, dsum = 0.f;
    bool have_local = false;
    auto sum_local = [&]() {
      asm volatile("" ::: "memory");
#pragma unroll
      for (int w = 0; w < NSW; ++w) {
        const f32x2 x = *(const LAS f32x2*)(size_t)(sm0 + RED_OFF + w * RED_STRIDE + lane * 8);
        n0 += x[0]; n1 += x[1]; dsum += *(const LAS float*)(size_t)(sm0 + RED_OFF + w * RED_STRIDE + 512);
      }
      have_local = true;
    };
    auto bump = [&]() -> bool {
      const u64 tn = now();
      if (owner && tn - tb > BUMP_T) { tb = tn; ++seq; if (lane == 0) st8(hrec + OFF_BEACON, seq, tag); }
      return tn - t0 > TMO;
    };
    if (owner) {
      asm volatile("s_waitcnt vmcnt(0)" : "+v"(a0), "+v"(a1), "+v"(a2), "+v"(a3), "+v"(ad) :: "memory");
      u32 cand = rfl(a0[1]) + 1u;
      while (__any(a0[1] == cand || a0[3] == cand || a1[1] == cand || a1[3] == cand || a2[1] == cand || a2[3] == cand ||
                   a3[1] == cand || a3[3] == cand || ad[1] == cand)) ++cand;
      tag = cand;
      if (lane == 0) st8(hrec + OFF_BEACON, seq, tag);
      tb = now();
    } else {
      asm volatile("s_waitcnt vmcnt(0)" : "+v"(a0) :: "memory");
      const u32 b0 = rfl(a0[0]), b1 = rfl(a0[1]);
      while (now() - t0 < READ_T && rfl(lds_ld(sm0 + CW_OFF + 8)) < TKN) __builtin_amdgcn_s_sleep(16);
      for (;;) {
        u64 x = 0;
        if (lane == 0) x = ld8(hrec + OFF_BEACON);
        tag = rfl((u32)(x >> 32));
        if (rfl((u32)x) != b0 || tag != b1 || now() - t0 > TMO) break;
        __builtin_amdgcn_s_sleep(32);
      }
    }
    STAMPV(1);
    while (rfl(lds_ld(sm0 + CW_OFF + 8)) < TKN || rfl(lds_ld(sm0 + CW_OFF + 12)) < TKN) { __builtin_amdgcn_s_sleep(4); if (bump()) break; }
    u32 tk = 0;
    if (lane == 0) tk = __hip_atomic_fetch_add((GAS u32*)(hrec + OFF_TICKET), 1u, __ATOMIC_RELAXED, __HIP_MEMORY_SCOPE_AGENT);
    const bool fin = rfl(tk) == 3u;
    STAMPV(2);
    if (fin) {
      const u32 j1 = (split + 1) & 3, j2 = (split + 2) & 3, j3 = (split + 3) & 3;
      auto poll_issue = [&](u32 buf) {
        dma1k_sc1(hrec + j1 * REC_B, lane * 16, buf + j1 * 1024); dma1k_sc1(hrec + j2 * REC_B, lane * 16, buf + j2 * 1024);
        dma1k_sc1(hrec + j3 * REC_B, lane * 16, buf + j3 * 1024);
        if (lane < 4) dma1k_sc1(hrec + OFF_DEN, lane * REC_B, buf + 4096);
      };
      u32 cur = sm0 + PB_OFF, oth = cur + PB_B;
      dma1k((const char*)(out + bh * (D / 2)), c * 16, cur + split * 1024);
      poll_issue(cur);
      __builtin_amdgcn_s_sleep(POLL_GAP);
#pragma unroll 1
      for (u32 it = 1;; ++it) {
        poll_issue(oth);
        if (!have_local && rfl(lds_ld(sm0 + CW_OFF)) == NSW) sum_local();
        wait_vm<4>();
        a0 = lds_rd128(cur + lane * 16); a1 = lds_rd128(cur + 1024 + lane * 16); a2 = lds_rd128(cur + 2048 + lane * 16); a3 = lds_rd128(cur + 3072 + lane * 16);
        ad = lds_rd128(cur + 4096 + (lane & 3) * 16);
        if (__all((split == 0 || (a0[1] == tag && a0[3] == tag)) && (split == 1 || (a1[1] == tag && a1[3] == tag)) &&
                  (split == 2 || (a2[1] == tag && a2[3] == tag)) && (split == 3 || (a3[1] == tag && a3[3] == tag)) &&
                  ((u32)(lane & 3) == split || ad[1] == tag))) break;
        if ((it & 7) == 0 && now() - t0 > TMO) break;
        const u32 x = cur; cur = oth; oth = x;
      }
    }
    STAMPV(3);
    bool all4 = !owner || fin;
    if (!have_local) {
      bool pending = false;
      const u32 land = sm0 + PB_OFF;
      for (u32 it = 1;; ++it) {
        const u32 dn = rfl(lds_ld(sm0 + CW_OFF));
        if (dn == NSW) break;
        if (!all4) {
          if (!pending) {
            if (lane == 0) { lds_st(land, ~0u); wait_lgkm0(); dma1k_sc1(hrec + OFF_TICKET, 0u, land); }
            pending = true;
          } else {
            const u32 x = rfl(lds_ld(land));
            if (x != ~0u) { pending = false; all4 = x == 4u; }
          }
          if (bump()) break;
        }
        if (dn == 0) __builtin_amdgcn_s_sleep(2);
        if ((it & 1023) == 0 && now() - t0 > TMO) break;
      }
      STAMPV(4);
      sum_local();
    }
    if (fin) {
      const float s0 = (((split == 0 ? n0 : __uint_as_float(a0[0])) + (split == 1 ? n0 : __uint_as_float(a1[0]))) +
                        (split == 2 ? n0 : __uint_as_float(a2[0]))) + (split == 3 ? n0 : __uint_as_float(a3[0]));
      const float s1 = (((split == 0 ? n1 : __uint_as_float(a0[2])) + (split == 1 ? n1 : __uint_as_float(a1[2]))) +
                        (split == 2 ? n1 : __uint_as_float(a2[2]))) + (split == 3 ? n1 : __uint_as_float(a3[2]));
      const u32 dl = (u32)(lane & 3) == split ? __float_as_uint(dsum) : ad[0];
      const float sd = ((__uint_as_float(__builtin_amdgcn_readlane(dl, 0)) + __uint_as_float(__builtin_amdgcn_readlane(dl, 1))) +
                        __uint_as_float(__builtin_amdgcn_readlane(dl, 2))) + __uint_as_float(__builtin_amdgcn_readlane(dl, 3));
      const float den = rbf(rbf(sd) + 1e-6f);
      out[bh * (D / 2) + lane] = pk_bf16(rbf(s0) / den, rbf(s1) / den);
      wait_vm<0>();
    } else {
      char* myrec = hrec + split * REC_B;
      st8(myrec + lane * 16, __float_as_uint(n0), tag);
      st8(myrec + lane * 16 + 8, __float_as_uint(n1), tag);
      if (lane == 0) st8(myrec + OFF_DEN, __float_as_uint(dsum), tag);
      if (!all4) {
        for (;;) {
          u32 x = 0;
          if (lane == 0) x = ld4(hrec + OFF_TICKET);
          if (rfl(x) == 4u || bump()) break;
          __builtin_amdgcn_s_sleep(8);
        }
      }
    }
  } else {
    const u32 cwv = wave - NLD;
    const u32 lds0 = sm0 + cwv * WAVE_LDS;
    const u32 aux = lds0 + NSLOT * TILE_B;
    const u32 lw = sm0 + CW_OFF + 8 + (cwv >> 1) * 4, fw = sm0 + CW_OFF + 16 + cwv * 4;
    if (lane == 0) lds_st(fw, 0u);
    asm volatile("s_waitcnt lgkmcnt(0)\n\ts_barrier" ::: "memory");
    u32 kra[4], vra[8];
#pragma unroll
    for (int i = 0; i < 4; ++i) kra[i] = lds0 + c * 256 + (((4 * i + g) ^ c) << 4);
    {
      const u32 row = 4 * g + (c >> 2);
#pragma unroll
      for (int b = 0; b < 8; ++b) vra[b] = lds0 + row * 256 + ((b ^ (row & 7)) << 5) + (c & 3) * 8;
    }
    const float oneX = c < 8 ? 1.0f : 0.0f, oneY = c < 8 ? 0.0f : 1.0f;
    wait_ge(sm0 + CW_OFF + 8, 1);
    u32x4 omX[4], omY[4];
#pragma unroll
    for (int i = 0; i < 4; ++i) {
      const u32x4 om = lds_rd128(stg + (c & 7) * 256 + (4 * i + g) * 16);
#pragma unroll
      for (int j = 0; j < 4; ++j) { omX[i][j] = c < 8 ? om[j] : 0u; omY[i][j] = c < 8 ? 0u : om[j]; }
    }
    STAMPV(2);
    u32 seen = 0, need = (cwv & 1) + 1;
#pragma unroll 1
    for (u32 p = 0; p < 8; ++p) {
      const u32 so = (p & 3) * (2 * TILE_B);
      if (seen < need) seen = wait_ge(lw, need);
      need += 2;
      u32x4 kx[4], ky[4];
#pragma unroll
      for (int i = 0; i < 4; ++i) kx[i] = lds_rd128(kra[i] + so);
#pragma unroll
      for (int i = 0; i < 4; ++i) ky[i] = lds_rd128(kra[i] + so + TILE_B);
      wait_lgkm0();
      PIN4(kx); PIN4(ky);
      if (lane == 0) lds_st(fw, p + 1);
      f32x4 pr = {0.f, 0.f, 0.f, 0.f};
      float sx = 0.f, sy = 0.f, sx2 = 0.f, sy2 = 0.f;
#pragma unroll
      for (int i = 0; i < 4; ++i) {
#pragma unroll
        for (int j = 0; j < 4; ++j) { kx[i][j] = scale_pk(kx[i][j], SCALE); if (j & 1) sx2 = dot2(kx[i][j], kx[i][j], sx2); else sx = dot2(kx[i][j], kx[i][j], sx); }
        pr = mfma32(kx[i], omX[i], pr);
      }
#pragma unroll
      for (int i = 0; i < 4; ++i) {
#pragma unroll
        for (int j = 0; j < 4; ++j) { ky[i][j] = scale_pk(ky[i][j], SCALE); if (j & 1) sy2 = dot2(ky[i][j], ky[i][j], sy2); else sy = dot2(ky[i][j], ky[i][j], sy); }
        pr = mfma32(ky[i], omY[i], pr);
      }
      f32x4 sq = {0.f, 0.f, 0.f, 0.f};
      sq = mfma4f(sx + sx2, oneX, sq);
      sq = mfma4f(sy + sy2, oneY, sq);
      const u32x2 ph = phi4(pr, sq);
      *(LAS u32x2*)(size_t)(aux + p * 512 + lane * 8) = ph;
    }
    STAMPV(3);
    f32x4 acc[8], zacc = {0.f, 0.f, 0.f, 0.f};
#pragma unroll
    for (int b = 0; b < 8; ++b) acc[b] = f32x4{0.f, 0.f, 0.f, 0.f};
    const u32x4 ones = {0x3f803f80u, 0x3f803f80u, 0x3f803f80u, 0x3f803f80u};
    wait_ge(sm0 + CW_OFF + 4, 1);
    const f32x4 phq = *(const LAS f32x4*)(size_t)(sm0 + CW_OFF + 32 + (g & 1) * 16);
#pragma unroll 1
    for (u32 p = 0; p < 8; ++p) {
      const u32 so = (p & 3) * (2 * TILE_B);
      const u32x2 ph = *(const LAS u32x2*)(size_t)(aux + p * 512 + lane * 8);
      u32x4 a;
      a[0] = c < 8 ? ph[0] : 0u; a[1] = c < 8 ? ph[1] : 0u; a[2] = c < 8 ? 0u : ph[0]; a[3] = c < 8 ? 0u : ph[1];
      asm volatile("" : "+v"(a));
      if (seen < need) seen = wait_ge(lw, need);
      need += 2;
      u32x2 vx[8], vy[8];
#pragma unroll
      for (int b = 0; b < 8; ++b) vx[b] = lds_tr(vra[b] + so);
#pragma unroll
      for (int b = 0; b < 8; ++b) vy[b] = lds_tr(vra[b] + so + TILE_B);
      zacc = mfma32(a, ones, zacc);
      wait_lgkm0();
      PIN8(vx); PIN8(vy);
      if (lane == 0) lds_st(fw, p + 9);
#pragma unroll
      for (int b = 0; b < 8; ++b) {
        const u32x4 bb = {vx[b][0], vx[b][1], vy[b][0], vy[b][1]};
        acc[b] = mfma32(a, bb, acc[b]);
      }
    }
    STAMPV(4);
    float t[8];
#pragma unroll
    for (int b = 0; b < 8; ++b)
      t[b] = fmaf(phq[3], acc[b][3], fmaf(phq[2], acc[b][2], fmaf(phq[1], acc[b][1], phq[0] * acc[b][0])));
    float dn = fmaf(phq[3], zacc[3], fmaf(phq[2], zacc[2], fmaf(phq[1], zacc[1], phq[0] * zacc[0])));
    float u[4];
#pragma unroll
    for (int b = 0; b < 4; ++b) {
      auto s = __builtin_amdgcn_permlane32_swap(__float_as_uint(t[b]), __float_as_uint(t[b + 4]), false, false);
      u[b] = __uint_as_float(s[0]) + __uint_as_float(s[1]);
    }
    float w2[2];
#pragma unroll
    for (int b = 0; b < 2; ++b) {
      auto s = __builtin_amdgcn_permlane16_swap(__float_as_uint(u[b]), __float_as_uint(u[b + 2]), false, false);
      w2[b] = __uint_as_float(s[0]) + __uint_as_float(s[1]);
    }
    {
      auto s = __builtin_amdgcn_permlane32_swap(__float_as_uint(dn), __float_as_uint(dn), false, false);
      dn = __uint_as_float(s[0]) + __uint_as_float(s[1]);
      auto s2 = __builtin_amdgcn_permlane16_swap(__float_as_uint(dn), __float_as_uint(dn), false, false);
      dn = __uint_as_float(s2[0]) + __uint_as_float(s2[1]);
    }
    LAS float* red = (LAS float*)(size_t)(sm0 + RED_OFF + cwv * RED_STRIDE);
    red[32 * g + c] = w2[0];
    red[32 * g + 16 + c] = w2[1];
    if (lane == 0) red[128] = dn;
    wait_lgkm0();
    if (lane == 0) lds_add(sm0 + CW_OFF, 1u);
  }
}

extern "C" void kernel_launch(void* const* d_in, const int* in_sizes, int n_in,
                              void* d_out, int out_size, void* d_ws, size_t ws_size,
                              hipStream_t stream) {
  rfa_main<<<NBH * 4, NWV * 64, 0, stream>>>((const char*)d_in[0], (const char*)d_in[1], (const char*)d_in[2],
                                                  (const char*)d_in[3], (char*)d_ws, (u32*)d_out);
}
```

```cpp
#include <hip/hip_runtime.h>
#include <hip/hip_bf16.h>
#include <stdint.h>

typedef unsigned int u32;
typedef unsigned long long u64;
using u32x2 = __attribute__((ext_vector_type(2))) unsigned;
using u32x4 = __attribute__((ext_vector_type(4))) unsigned;
using f32x2 = __attribute__((ext_vector_type(2))) float;
using f32x4 = __attribute__((ext_vector_type(4))) float;
using bf16x2 = __attribute__((ext_vector_type(2))) __bf16;
using bf16x8 = __attribute__((ext_vector_type(8))) __bf16;
using s16x4 = __attribute__((ext_vector_type(4))) short;
#define LAS __attribute__((address_space(3)))
#define GAS __attribute__((address_space(1)))

constexpr int NBH = 64, SK = 4096, D = 128, M = 8;
constexpr int NSW = 4;
constexpr int TILE_B = 4096;
constexpr int NSLOT = 8;
constexpr int WIN = 6;
constexpr int NLD = 2;
constexpr int NWV = NSW + NLD + 1;
constexpr int AUX_B = 4096;
constexpr int WAVE_LDS = NSLOT * TILE_B + AUX_B;
constexpr int RED_OFF = NSW * WAVE_LDS, RED_STRIDE = 576;
constexpr int CW_OFF = RED_OFF + NSW * RED_STRIDE;
constexpr int STG_OFF = CW_OFF + 64;
constexpr int PB_OFF = STG_OFF + 3072, PB_B = 4096 + 64;
constexpr int LDS_BYTES = PB_OFF + 2 * PB_B;
constexpr size_t TSTEP = (size_t)NSW * TILE_B;
constexpr u32 WG_B = SK / 4 * D * 2;
constexpr int REC_B = 2048, HEAD_B = 5 * REC_B;
constexpr int OFF_DEN = 1024, OFF_BEACON = 4 * REC_B, OFF_TICKET = 4 * REC_B + 1024;

__device__ __forceinline__ float bf_lo(u32 x) { return __uint_as_float(x << 16); }
__device__ __forceinline__ float bf_hi(u32 x) { return __uint_as_float(x & 0xffff0000u); }
__device__ __forceinline__ u32 pk_bf16(float a, float b) {
  f32x2 f = {a, b};
  return __builtin_bit_cast(u32, __builtin_convertvector(f, bf16x2));
}
__device__ __forceinline__ u32 scale_pk(u32 x, float s) {
  f32x2 f = {bf_lo(x), bf_hi(x)};
  f = f * s;
  return __builtin_bit_cast(u32, __builtin_convertvector(f, bf16x2));
}
__device__ __forceinline__ float dot2(u32 a, u32 b, float c) {
  return __builtin_amdgcn_fdot2_f32_bf16(__builtin_bit_cast(bf16x2, a), __builtin_bit_cast(bf16x2, b), c, false);
}
__device__ __forceinline__ f32x4 mfma32(u32x4 a, u32x4 b, f32x4 c) {
  return __builtin_amdgcn_mfma_f32_16x16x32_bf16(__builtin_bit_cast(bf16x8, a), __builtin_bit_cast(bf16x8, b), c, 0, 0, 0);
}
__device__ __forceinline__ f32x4 mfma4f(float a, float b, f32x4 c) { return __builtin_amdgcn_mfma_f32_16x16x4f32(a, b, c, 0, 0, 0); }

__device__ __forceinline__ void fill4k(const char* sbase, const u32 (&voff)[4], u32 lds) {
  u32 keep;
  asm volatile("s_mov_b32 %0, m0\n\ts_mov_b32 m0, %6\n\ts_nop 0\n\t"
               "global_load_lds_dwordx4 %1, %5 offset:0 nt\n\t"
               "global_load_lds_dwordx4 %2, %5 offset:1024 nt\n\t"
               "global_load_lds_dwordx4 %3, %5 offset:2048 nt\n\t"
               "global_load_lds_dwordx4 %4, %5 offset:3072 nt\n\t"
               "s_mov_b32 m0, %0"
               : "=&s"(keep) : "v"(voff[0]), "v"(voff[1]), "v"(voff[2]), "v"(voff[3]), "s"(sbase), "s"(lds) : "memory");
}
__device__ __forceinline__ void dma1k(const char* sbase, u32 voff, u32 lds) {
  u32 keep;
  asm volatile("s_mov_b32 %0, m0\n\ts_mov_b32 m0, %3\n\ts_nop 0\n\t"
               "global_load_lds_dwordx4 %1, %2\n\t"
               "s_mov_b32 m0, %0"
               : "=&s"(keep) : "v"(voff), "s"(sbase), "s"(lds) : "memory");
}
__device__ __forceinline__ void dma1k_sc1(const char* sbase, u32 voff, u32 lds) {
  u32 keep;
  asm volatile("s_mov_b32 %0, m0\n\ts_mov_b32 m0, %3\n\ts_nop 0\n\t"
               "global_load_lds_dwordx4 %1, %2 sc1\n\t"
               "s_mov_b32 m0, %0"
               : "=&s"(keep) : "v"(voff), "s"(sbase), "s"(lds) : "memory");
}
template <int OFF> __device__ __forceinline__ u32x4 gld16_sc1(const char* sbase, u32 voff) {
  u32x4 r;
  asm volatile("global_load_dwordx4 %0, %1, %2 offset:%3 sc1" : "=v"(r) : "v"(voff), "s"(sbase), "i"(OFF) : "memory");
  return r;
}
__device__ __forceinline__ u32 lds_ld(u32 addr) { return __hip_atomic_load((LAS u32*)(size_t)addr, __ATOMIC_RELAXED, __HIP_MEMORY_SCOPE_WORKGROUP); }
__device__ __forceinline__ void lds_st(u32 addr, u32 v) { __hip_atomic_store((LAS u32*)(size_t)addr, v, __ATOMIC_RELAXED, __HIP_MEMORY_SCOPE_WORKGROUP); }
__device__ __forceinline__ u32 lds_add(u32 addr, u32 v) { return __hip_atomic_fetch_add((LAS u32*)(size_t)addr, v, __ATOMIC_RELAXED, __HIP_MEMORY_SCOPE_WORKGROUP); }
__device__ __forceinline__ u32 rfl(u32 x) { return __builtin_amdgcn_readfirstlane(x); }
__device__ __forceinline__ u64 now() { return __builtin_amdgcn_s_memrealtime(); }
constexpr int POLL_GAP = 8;
constexpr u32 TKN = 22;
constexpr u64 BUMP_T = 300ull;
constexpr u64 READ_T = 1000ull;
constexpr u64 TMO = 20000000ull;
__device__ __forceinline__ void st8(char* p, u32 lo, u32 hi) {
  __hip_atomic_store((GAS u64*)p, ((u64)hi << 32) | lo, __ATOMIC_RELAXED, __HIP_MEMORY_SCOPE_AGENT);
}
__device__ __forceinline__ void st4(char* p, u32 v) { __hip_atomic_store((GAS u32*)p, v, __ATOMIC_RELAXED, __HIP_MEMORY_SCOPE_AGENT); }
__device__ __forceinline__ u64 ld8(const char* p) { return __hip_atomic_load((GAS u64*)p, __ATOMIC_RELAXED, __HIP_MEMORY_SCOPE_AGENT); }
__device__ __forceinline__ u32 ld4(const char* p) { return __hip_atomic_load((GAS u32*)p, __ATOMIC_RELAXED, __HIP_MEMORY_SCOPE_AGENT); }
__device__ __forceinline__ float rbf(float x) { return bf_lo(pk_bf16(x, x)); }
template <int N> __device__ __forceinline__ void wait_vm() { asm volatile("s_waitcnt vmcnt(%0)" :: "i"(N) : "memory"); }
__device__ __forceinline__ void wait_lgkm0() { asm volatile("s_waitcnt lgkmcnt(0)" ::: "memory"); }
#define PIN4(x) asm volatile("" : "+v"(x[0]), "+v"(x[1]), "+v"(x[2]), "+v"(x[3]) :: "memory")
#define PIN8(x) asm volatile("" : "+v"(x[0]), "+v"(x[1]), "+v"(x[2]), "+v"(x[3]), "+v"(x[4]), "+v"(x[5]), "+v"(x[6]), "+v"(x[7]) :: "memory")
__device__ __forceinline__ u32x4 lds_rd128(u32 addr) { return *(const LAS u32x4*)(size_t)addr; }
__device__ __forceinline__ u32x2 lds_tr(u32 addr) {
  return __builtin_bit_cast(u32x2, __builtin_amdgcn_ds_read_tr16_b64_v4i16((LAS s16x4*)(size_t)addr));
}

__device__ __forceinline__ u32x2 phi4(f32x4 pr, f32x4 sq) {
  const u32 p01 = pk_bf16(pr[0], pr[1]), p23 = pk_bf16(pr[2], pr[3]);
  const u32 s01 = pk_bf16(sq[0], sq[1]), s23 = pk_bf16(sq[2], sq[3]);
  const u32 x01 = pk_bf16(fmaf(-0.5f, bf_lo(s01), bf_lo(p01)), fmaf(-0.5f, bf_hi(s01), bf_hi(p01)));
  const u32 x23 = pk_bf16(fmaf(-0.5f, bf_lo(s23), bf_lo(p23)), fmaf(-0.5f, bf_hi(s23), bf_hi(p23)));
  const float L2E = 1.4426950408889634f;
  const u32 e01 = pk_bf16(__builtin_amdgcn_exp2f(bf_lo(x01) * L2E), __builtin_amdgcn_exp2f(bf_hi(x01) * L2E));
  const u32 e23 = pk_bf16(__builtin_amdgcn_exp2f(bf_lo(x23) * L2E), __builtin_amdgcn_exp2f(bf_hi(x23) * L2E));
  const float R = 0.35355339059327373f;
  const u32 y01 = pk_bf16(bf_lo(e01) * R, bf_hi(e01) * R), y23 = pk_bf16(bf_lo(e23) * R, bf_hi(e23) * R);
  u32x2 o;
  o[0] = pk_bf16(bf_lo(y01) + 1e-6f, bf_hi(y01) + 1e-6f);
  o[1] = pk_bf16(bf_lo(y23) + 1e-6f, bf_hi(y23) + 1e-6f);
  return o;
}

template <int R> __device__ __forceinline__ void drain(u32 word, u32& landed, int lane) {
  wait_vm<8 * R>();
  ++landed;
  if (lane == 0) lds_st(word, landed);
  if constexpr (R > 0) drain<R - 1>(word, landed, lane);
}
__device__ __forceinline__ u32 wait_ge(u32 word, u32 need) {
  u32 v = rfl(lds_ld(word));
  for (u32 it = 1; v < need; ++it) {
    v = rfl(lds_ld(word));
    if (it == (1u << 24)) break;
  }
  asm volatile("" ::: "memory");
  return v;
}
__global__ void __launch_bounds__(NWV * 64, 2)
rfa_main(const char* __restrict__ qg, const char* __restrict__ kg, const char* __restrict__ vg, const char* __restrict__ og,
         char* __restrict__ ws, u32* __restrict__ out) {
  __shared__ __attribute__((aligned(16))) char smem[LDS_BYTES];
  const int lane = threadIdx.x & 63, wave = __builtin_amdgcn_readfirstlane(threadIdx.x >> 6);
  const int g = lane >> 4, c = lane & 15;
  const u32 bh = ((blockIdx.x >> 2) & 1) * 32 + (blockIdx.x >> 3), split = blockIdx.x & 3;
  const u32 sm0 = (u32)(size_t)(LAS char*)smem;
  const u32 stg = sm0 + STG_OFF;
  const float SCALE = 0.29730177875068026f;
  u32 koff[4];
#pragma unroll
  for (int p = 0; p < 4; ++p) koff[p] = g * 256 + ((c ^ (4 * p + g)) << 4);
  const size_t hoff = (size_t)bh * (SK * D * 2) + split * WG_B + wave * (2 * TILE_B);
  asm volatile("" :: "s"(qg), "s"(kg), "s"(vg), "s"(og), "s"(ws), "s"(out), "v"(koff[0]), "v"(koff[1]), "v"(koff[2]), "v"(koff[3]), "s"(hoff));

  if (__builtin_expect(wave < NLD, 1)) {
    const char* kb = kg + hoff;
    const char* vb = vg + hoff;
    const u32 ring = sm0 + wave * (2 * WAVE_LDS);
    const u32 lw = sm0 + CW_OFF + 8 + wave * 4;
    if (wave == 0) { dma1k(og, lane * 16, stg); dma1k(og + 1024, lane * 16, stg + 1024); }
    else dma1k(qg + bh * (D * 2), c * 16, stg + 2048);
#pragma unroll
    for (int n = 0; n < WIN - 1; ++n) {
      fill4k(kb + (n & 1) * TILE_B + (n >> 1) * (2 * TSTEP), koff, ring + (n & 1) * WAVE_LDS + (n >> 1) * (2 * TILE_B));
      fill4k(kb + (n & 1) * TILE_B + (n >> 1) * (2 * TSTEP) + TSTEP, koff, ring + (n & 1) * WAVE_LDS + (n >> 1) * (2 * TILE_B) + TILE_B);
    }
    if (lane == 0) lds_st(lw, 0u);
    asm volatile("s_waitcnt lgkmcnt(0)\n\ts_barrier" ::: "memory");
    __builtin_amdgcn_s_setprio(2);
    u32 voffs[4];
#pragma unroll
    for (int p = 0; p < 4; ++p) voffs[p] = g * 256 + ((c ^ (((4 * p + g) & 7) << 1)) << 4);
    u32 landed = 0, fr0 = 0, fr1 = 0;
#pragma unroll 1
    for (u32 n = WIN - 1; n < 32; ++n) {
      const u32 cw = n & 1, j = n >> 1;
      if (j >= 4) {
        if (cw == 0) { if (fr0 < j - 3) fr0 = wait_ge(sm0 + CW_OFF + 16 + wave * 8, j - 3); }
        else { if (fr1 < j - 3) fr1 = wait_ge(sm0 + CW_OFF + 20 + wave * 8, j - 3); }
      }
      const bool isk = j < 8;
      const char* src = (isk ? kb : vb) + cw * TILE_B + (j & 7) * (u32)(2 * TSTEP);
      u32 no[4];
#pragma unroll
      for (int i = 0; i < 4; ++i) no[i] = isk ? koff[i] : voffs[i];
      const u32 dst = ring + cw * WAVE_LDS + (j & 3) * (2 * TILE_B);
      fill4k(src, no, dst);
      fill4k(src + TSTEP, no, dst + TILE_B);
      wait_vm<8 * (WIN - 1)>();
      ++landed;
      if (lane == 0) lds_st(lw, landed);
    }
    drain<WIN - 2>(lw, landed, lane);
  } else if (__builtin_expect(wave == NWV - 1, 1)) {
    char* hrec = ws + (size_t)bh * HEAD_B;
    const bool owner = split == 0;
    u32x4 a0, a1, a2, a3, ad;
    if (owner) {
      a0 = gld16_sc1<0>(hrec, lane * 16); a1 = gld16_sc1<REC_B>(hrec, lane * 16);
      a2 = gld16_sc1<0>(hrec + 2 * REC_B, lane * 16); a3 = gld16_sc1<REC_B>(hrec + 2 * REC_B, lane * 16);
      ad = gld16_sc1<OFF_DEN>(hrec, (lane & 3) * REC_B);
      if (lane == 0) st4(hrec + OFF_TICKET, 0u);
    } else {
      a0 = gld16_sc1<0>(hrec + OFF_BEACON, 0u);
    }
    if (lane < 2) lds_st(sm0 + CW_OFF + lane * 4, 0u);
    const u64 t0 = now();
    asm volatile("s_waitcnt lgkmcnt(0)\n\ts_barrier" ::: "memory");
    {
      wait_ge(sm0 + CW_OFF + 8, 1); wait_ge(sm0 + CW_OFF + 12, 1);
      f32x4 qp = {0.f, 0.f, 0.f, 0.f};
      float qs = 0.f, qs2 = 0.f;
#pragma unroll
      for (int i = 0; i < 4; ++i) {
        const u32x4 om = lds_rd128(stg + (c & 7) * 256 + (4 * i + g) * 16);
        u32x4 qf = lds_rd128(stg + 2048 + (4 * i + g) * 16);
#pragma unroll
        for (int j = 0; j < 4; ++j) {
          qf[j] = scale_pk(qf[j], SCALE);
          if (j & 1) qs2 = dot2(qf[j], qf[j], qs2); else qs = dot2(qf[j], qf[j], qs);
        }
        qp = mfma32(om, qf, qp);
      }
      f32x4 qq = {0.f, 0.f, 0.f, 0.f};
      qq = mfma4f(1.0f, qs + qs2, qq);
      const u32x2 pq = phi4(qp, qq);
      if (c == 0 && g < 2) *(LAS f32x4*)(size_t)(sm0 + CW_OFF + 32 + g * 16) = f32x4{bf_lo(pq[0]), bf_hi(pq[0]), bf_lo(pq[1]), bf_hi(pq[1])};
      wait_lgkm0();
      if (lane == 0) lds_st(sm0 + CW_OFF + 4, 1u);
    }
    __builtin_amdgcn_s_setprio(3);
    u32 tag, seq = 1;
    u64 tb = 0;
    float n0 = 0.f, n1 = 0.f, dsum = 0.f;
    bool have_local = false;
    auto sum_local = [&]() {
      asm volatile("" ::: "memory");
#pragma unroll
      for (int w = 0; w < NSW; ++w) {
        const f32x2 x = *(const LAS f32x2*)(size_t)(sm0 + RED_OFF + w * RED_STRIDE + lane * 8);
        n0 += x[0]; n1 += x[1]; dsum += *(const LAS float*)(size_t)(sm0 + RED_OFF + w * RED_STRIDE + 512);
      }
      have_local = true;
    };
    auto bump = [&]() -> bool {
      const u64 tn = now();
      if (owner && tn - tb > BUMP_T) { tb = tn; ++seq; if (lane == 0) st8(hrec + OFF_BEACON, seq, tag); }
      return tn - t0 > TMO;
    };
    if (owner) {
      asm volatile("s_waitcnt vmcnt(0)" : "+v"(a0), "+v"(a1), "+v"(a2), "+v"(a3), "+v"(ad) :: "memory");
      u32 cand = rfl(a0[1]) + 1u;
      while (__any(a0[1] == cand || a0[3] == cand || a1[1] == cand || a1[3] == cand || a2[1] == cand || a2[3] == cand ||
                   a3[1] == cand || a3[3] == cand || ad[1] == cand)) ++cand;
      tag = cand;
      if (lane == 0) st8(hrec + OFF_BEACON, seq, tag);
      tb = now();
    } else {
      asm volatile("s_waitcnt vmcnt(0)" : "+v"(a0) :: "memory");
      const u32 b0 = rfl(a0[0]), b1 = rfl(a0[1]);
      while (now() - t0 < READ_T && rfl(lds_ld(sm0 + CW_OFF + 8)) < TKN) __builtin_amdgcn_s_sleep(16);
      for (;;) {
        u64 x = 0;
        if (lane == 0) x = ld8(hrec + OFF_BEACON);
        tag = rfl((u32)(x >> 32));
        if (rfl((u32)x) != b0 || tag != b1 || now() - t0 > TMO) break;
        __builtin_amdgcn_s_sleep(32);
      }
    }
    while (rfl(lds_ld(sm0 + CW_OFF + 8)) < TKN || rfl(lds_ld(sm0 + CW_OFF + 12)) < TKN) { __builtin_amdgcn_s_sleep(4); if (bump()) break; }
    u32 tk = 0;
    if (lane == 0) tk = __hip_atomic_fetch_add((GAS u32*)(hrec + OFF_TICKET), 1u, __ATOMIC_RELAXED, __HIP_MEMORY_SCOPE_AGENT);
    const bool fin = rfl(tk) == 3u;
    if (fin) {
      const u32 j1 = (split + 1) & 3, j2 = (split + 2) & 3, j3 = (split + 3) & 3;
      auto poll_issue = [&](u32 buf) {
        dma1k_sc1(hrec + j1 * REC_B, lane * 16, buf + j1 * 1024); dma1k_sc1(hrec + j2 * REC_B, lane * 16, buf + j2 * 1024);
        dma1k_sc1(hrec + j3 * REC_B, lane * 16, buf + j3 * 1024);
        if (lane < 4) dma1k_sc1(hrec + OFF_DEN, lane * REC_B, buf + 4096);
      };
      u32 cur = sm0 + PB_OFF, oth = cur + PB_B;
      dma1k((const char*)(out + bh * (D / 2)), c * 16, cur + split * 1024);
      poll_issue(cur);
      __builtin_amdgcn_s_sleep(POLL_GAP);
#pragma unroll 1
      for (u32 it = 1;; ++it) {
        poll_issue(oth);
        if (!have_local && rfl(lds_ld(sm0 + CW_OFF)) == NSW) sum_local();
        wait_vm<4>();
        a0 = lds_rd128(cur + lane * 16); a1 = lds_rd128(cur + 1024 + lane * 16); a2 = lds_rd128(cur + 2048 + lane * 16); a3 = lds_rd128(cur + 3072 + lane * 16);
        ad = lds_rd128(cur + 4096 + (lane & 3) * 16);
        if (__all((split == 0 || (a0[1] == tag && a0[3] == tag)) && (split == 1 || (a1[1] == tag && a1[3] == tag)) &&
                  (split == 2 || (a2[1] == tag && a2[3] == tag)) && (split == 3 || (a3[1] == tag && a3[3] == tag)) &&
                  ((u32)(lane & 3) == split || ad[1] == tag))) break;
        if ((it & 7) == 0 && now() - t0 > TMO) break;
        const u32 x = cur; cur = oth; oth = x;
      }
    }
    bool all4 = !owner || fin;
    if (!have_local) {
      bool pending = false;
      const u32 land = sm0 + PB_OFF;
      for (u32 it = 1;; ++it) {
        const u32 dn = rfl(lds_ld(sm0 + CW_OFF));
        if (dn == NSW) break;
        if (!all4) {
          if (!pending) {
            if (lane == 0) { lds_st(land, ~0u); wait_lgkm0(); dma1k_sc1(hrec + OFF_TICKET, 0u, land); }
            pending = true;
          } else {
            const u32 x = rfl(lds_ld(land));
            if (x != ~0u) { pending = false; all4 = x == 4u; }
          }
          if (bump()) break;
        }
        if (dn == 0) __builtin_amdgcn_s_sleep(2);
        if ((it & 1023) == 0 && now() - t0 > TMO) break;
      }
      sum_local();
    }
    if (fin) {
      const float s0 = (((split == 0 ? n0 : __uint_as_float(a0[0])) + (split == 1 ? n0 : __uint_as_float(a1[0]))) +
                        (split == 2 ? n0 : __uint_as_float(a2[0]))) + (split == 3 ? n0 : __uint_as_float(a3[0]));
      const float s1 = (((split == 0 ? n1 : __uint_as_float(a0[2])) + (split == 1 ? n1 : __uint_as_float(a1[2]))) +
                        (split == 2 ? n1 : __uint_as_float(a2[2]))) + (split == 3 ? n1 : __uint_as_float(a3[2]));
      const u32 dl = (u32)(lane & 3) == split ? __float_as_uint(dsum) : ad[0];
      const float sd = ((__uint_as_float(__builtin_amdgcn_readlane(dl, 0)) + __uint_as_float(__builtin_amdgcn_readlane(dl, 1))) +
                        __uint_as_float(__builtin_amdgcn_readlane(dl, 2))) + __uint_as_float(__builtin_amdgcn_readlane(dl, 3));
      const float den = rbf(rbf(sd) + 1e-6f);
      out[bh * (D / 2) + lane] = pk_bf16(rbf(s0) / den, rbf(s1) / den);
      wait_vm<0>();
    } else {
      char* myrec = hrec + split * REC_B;
      st8(myrec + lane * 16, __float_as_uint(n0), tag);
      st8(myrec + lane * 16 + 8, __float_as_uint(n1), tag);
      if (lane == 0) st8(myrec + OFF_DEN, __float_as_uint(dsum), tag);
      if (!all4) {
        for (;;) {
          u32 x = 0;
          if (lane == 0) x = ld4(hrec + OFF_TICKET);
          if (rfl(x) == 4u || bump()) break;
          __builtin_amdgcn_s_sleep(8);
        }
      }
    }
  } else {
    const u32 cwv = wave - NLD;
    const u32 lds0 = sm0 + cwv * WAVE_LDS;
    const u32 aux = lds0 + NSLOT * TILE_B;
    const u32 lw = sm0 + CW_OFF + 8 + (cwv >> 1) * 4, fw = sm0 + CW_OFF + 16 + cwv * 4;
    if (lane == 0) lds_st(fw, 0u);
    asm volatile("s_waitcnt lgkmcnt(0)\n\ts_barrier" ::: "memory");
    u32 kra[4], vra[8];
#pragma unroll
    for (int i = 0; i < 4; ++i) kra[i] = lds0 + c * 256 + (((4 * i + g) ^ c) << 4);
    {
      const u32 row = 4 * g + (c >> 2);
#pragma unroll
      for (int b = 0; b < 8; ++b) vra[b] = lds0 + row * 256 + ((b ^ (row & 7)) << 5) + (c & 3) * 8;
    }
    const float oneX = c < 8 ? 1.0f : 0.0f, oneY = c < 8 ? 0.0f : 1.0f;
    wait_ge(sm0 + CW_OFF + 8, 1);
    u32x4 omX[4], omY[4];
#pragma unroll
    for (int i = 0; i < 4; ++i) {
      const u32x4 om = lds_rd128(stg + (c & 7) * 256 + (4 * i + g) * 16);
#pragma unroll
      for (int j = 0; j < 4; ++j) { omX[i][j] = c < 8 ? om[j] : 0u; omY[i][j] = c < 8 ? 0u : om[j]; }
    }
    u32 seen = 0, need = (cwv & 1) + 1;
#pragma unroll 1
    for (u32 p = 0; p < 8; ++p) {
      const u32 so = (p & 3) * (2 * TILE_B);
      if (seen < need) seen = wait_ge(lw, need);
      need += 2;
      u32x4 kx[4], ky[4];
#pragma unroll
      for (int i = 0; i < 4; ++i) kx[i] = lds_rd128(kra[i] + so);
#pragma unroll
      for (int i = 0; i < 4; ++i) ky[i] = lds_rd128(kra[i] + so + TILE_B);
      wait_lgkm0();
      PIN4(kx); PIN4(ky);
      if (lane == 0) lds_st(fw, p + 1);
      f32x4 pr = {0.f, 0.f, 0.f, 0.f};
      float sx = 0.f, sy = 0.f, sx2 = 0.f, sy2 = 0.f;
#pragma unroll
      for (int i = 0; i < 4; ++i) {
#pragma unroll
        for (int j = 0; j < 4; ++j) { kx[i][j] = scale_pk(kx[i][j], SCALE); if (j & 1) sx2 = dot2(kx[i][j], kx[i][j], sx2); else sx = dot2(kx[i][j], kx[i][j], sx); }
        pr = mfma32(kx[i], omX[i], pr);
      }
#pragma unroll
      for (int i = 0; i < 4; ++i) {
#pragma unroll
        for (int j = 0; j < 4; ++j) { ky[i][j] = scale_pk(ky[i][j], SCALE); if (j & 1) sy2 = dot2(ky[i][j], ky[i][j], sy2); else sy = dot2(ky[i][j], ky[i][j], sy); }
        pr = mfma32(ky[i], omY[i], pr);
      }
      f32x4 sq = {0.f, 0.f, 0.f, 0.f};
      sq = mfma4f(sx + sx2, oneX, sq);
      sq = mfma4f(sy + sy2, oneY, sq);
      const u32x2 ph = phi4(pr, sq);
      *(LAS u32x2*)(size_t)(aux + p * 512 + lane * 8) = ph;
    }
    f32x4 acc[8], zacc = {0.f, 0.f, 0.f, 0.f};
#pragma unroll
    for (int b = 0; b < 8; ++b) acc[b] = f32x4{0.f, 0.f, 0.f, 0.f};
    const u32x4 ones = {0x3f803f80u, 0x3f803f80u, 0x3f803f80u, 0x3f803f80u};
    wait_ge(sm0 + CW_OFF + 4, 1);
    const f32x4 phq = *(const LAS f32x4*)(size_t)(sm0 + CW_OFF + 32 + (g & 1) * 16);
#pragma unroll 1
    for (u32 p = 0; p < 8; ++p) {
      const u32 so = (p & 3) * (2 * TILE_B);
      const u32x2 ph = *(const LAS u32x2*)(size_t)(aux + p * 512 + lane * 8);
      u32x4 a;
      a[0] = c < 8 ? ph[0] : 0u; a[1] = c < 8 ? ph[1] : 0u; a[2] = c < 8 ? 0u : ph[0]; a[3] = c < 8 ? 0u : ph[1];
      asm volatile("" : "+v"(a));
      if (seen < need) seen = wait_ge(lw, need);
      need += 2;
      u32x2 vx[8], vy[8];
#pragma unroll
      for (int b = 0; b < 8; ++b) vx[b] = lds_tr(vra[b] + so);
#pragma unroll
      for (int b = 0; b < 8; ++b) vy[b] = lds_tr(vra[b] + so + TILE_B);
      zacc = mfma32(a, ones, zacc);
      wait_lgkm0();
      PIN8(vx); PIN8(vy);
      if (lane == 0) lds_st(fw, p + 9);
#pragma unroll
      for (int b = 0; b < 8; ++b) {
        const u32x4 bb = {vx[b][0], vx[b][1], vy[b][0], vy[b][1]};
        acc[b] = mfma32(a, bb, acc[b]);
      }
    }
    float t[8];
#pragma unroll
    for (int b = 0; b < 8; ++b)
      t[b] = fmaf(phq[3], acc[b][3], fmaf(phq[2], acc[b][2], fmaf(phq[1], acc[b][1], phq[0] * acc[b][0])));
    float dn = fmaf(phq[3], zacc[3], fmaf(phq[2], zacc[2], fmaf(phq[1], zacc[1], phq[0] * zacc[0])));
    float u[4];
#pragma unroll
    for (int b = 0; b < 4; ++b) {
      auto s = __builtin_amdgcn_permlane32_swap(__float_as_uint(t[b]), __float_as_uint(t[b + 4]), false, false);
      u[b] = __uint_as_float(s[0]) + __uint_as_float(s[1]);
    }
    float w2[2];
#pragma unroll
    for (int b = 0; b < 2; ++b) {
      auto s = __builtin_amdgcn_permlane16_swap(__float_as_uint(u[b]), __float_as_uint(u[b + 2]), false, false);
      w2[b] = __uint_as_float(s[0]) + __uint_as_float(s[1]);
    }
    {
      auto s = __builtin_amdgcn_permlane32_swap(__float_as_uint(dn), __float_as_uint(dn), false, false);
      dn = __uint_as_float(s[0]) + __uint_as_float(s[1]);
      auto s2 = __builtin_amdgcn_permlane16_swap(__float_as_uint(dn), __float_as_uint(dn), false, false);
      dn = __uint_as_float(s2[0]) + __uint_as_float(s2[1]);
    }
    LAS float* red = (LAS float*)(size_t)(sm0 + RED_OFF + cwv * RED_STRIDE);
    red[32 * g + c] = w2[0];
    red[32 * g + 16 + c] = w2[1];
    if (lane == 0) red[128] = dn;
    wait_lgkm0();
    if (lane == 0) lds_add(sm0 + CW_OFF, 1u);
  }
}

extern "C" void kernel_launch(void* const* d_in, const int* in_sizes, int n_in,
                              void* d_out, int out_size, void* d_ws, size_t ws_size,
                              hipStream_t stream) {
  rfa_main<<<NBH * 4, NWV * 64, 0, stream>>>((const char*)d_in[0], (const char*)d_in[1], (const char*)d_in[2],
                                                  (const char*)d_in[3], (char*)d_ws, (u32*)d_out);
}
```
